```python
import torch

def _softmax(x, axis=-1):
    return torch.softmax(x, dim=axis)
def _gelu(x):
    return torch.nn.functional.gelu(x, approximate="tanh")
def _silu(x):
    return torch.nn.functional.silu(x)
def _sigmoid(x):
    return torch.sigmoid(x)
def _layernorm(x, axis=-1, eps=1e-5):
    m = x.mean(dim=axis, keepdim=True)
    v = x.var(dim=axis, keepdim=True, unbiased=False)
    return (x - m) / torch.sqrt(v + eps)
def _scatter_add(tgt, idx, src, axis=0):
    return tgt.clone().index_add_(axis, idx, src)
def _segment_sum(src, seg, n):
    out = torch.zeros((n,) + src.shape[1:], dtype=src.dtype)
    return out.index_add_(0, seg, src)
def _pad(x, pw):
    flat = tuple(v for pair in reversed(pw) for v in pair)
    return torch.nn.functional.pad(x, flat)

kernel_name = 'gated_mix__gqa_block_cached__geglu_ffn__decode'

def setup_inputs():

    _w = (lambda a: (lambda t: t.to(torch.bfloat16) if t.is_floating_point() else t)(torch.as_tensor(a)))
    def _A_setup():
        B, Sq, D = 2, 1, 576
        Hq, Hkv, Sk, Dh = 32, 16, 32768, 128
        assert Hq % Hkv == 0, (Hq, Hkv)
        return {
            "x": _w((lambda *s: torch.randn(*s, dtype=torch.bfloat16) * 0.05)(B, Sq, D)),
            "k_cache": _w((lambda *s: torch.randn(*s, dtype=torch.bfloat16) * 6.4)(B, Hkv, Sk, Dh)),
            "v_cache": _w((lambda *s: torch.randn(*s, dtype=torch.bfloat16) * 0.4)(B, Hkv, Sk, Dh)),
            "wq": _w((lambda *s: torch.randn(*s, dtype=torch.bfloat16) * 6.4)(D, Hq * Dh) * (D ** -0.5)),
            "wo": _w((lambda *s: torch.randn(*s, dtype=torch.bfloat16) * 0.4)(Hq * Dh, D) * ((Hq * Dh) ** -0.5)),
        }
    _A_in = _A_setup()

    def _B_setup():
        B, S, D, F = 2, 1, 576, 4608
        return {
            "x": _w((lambda *s: torch.randn(*s, dtype=torch.bfloat16) * 0.05)(B, S, D)),
            "w_gate": _w((lambda *s: torch.randn(*s, dtype=torch.bfloat16) * 0.05)(D, F)),
            "w_up": _w((lambda *s: torch.randn(*s, dtype=torch.bfloat16) * 0.05)(D, F)),
            "w_down": _w((lambda *s: torch.randn(*s, dtype=torch.bfloat16) * 0.05)(F, D)),
        }
    _B_in = _B_setup()

    return {
        "A_x": _A_in["x"],
        "A_k_cache": _A_in["k_cache"],
        "A_v_cache": _A_in["v_cache"],
        "A_wq": _A_in["wq"],
        "A_wo": _A_in["wo"],
        "B_x": _B_in["x"],
        "B_w_gate": _B_in["w_gate"],
        "B_w_up": _B_in["w_up"],
        "B_w_down": _B_in["w_down"],
        "gate_w": _w((lambda *s: torch.randn(*s, dtype=torch.bfloat16) * 0.05)(576) * 16.0),
    }

def reference(A_x, A_k_cache, A_v_cache, A_wq, A_wo, B_x, B_w_gate, B_w_up, B_w_down, gate_w):
    def _A_ref(x, k_cache, v_cache, wq, wo):
        B, Sq, D = x.shape
        Hq, Dh = 32, 128
        Hkv = k_cache.shape[1]
        rep = Hq // Hkv
        q = (lambda x, *d: x.permute(*d))((x @ wq).reshape(B, Sq, Hq, Dh), 0, 2, 1, 3)
        k = (lambda x, n, axis: torch.repeat_interleave(x, n, dim=axis))(k_cache, rep, axis=1)
        v = (lambda x, n, axis: torch.repeat_interleave(x, n, dim=axis))(v_cache, rep, axis=1)
        scores = torch.einsum("bhqd,bhkd->bhqk", q, k) * (Dh ** -0.5)
        p = _softmax(scores, axis=-1)
        o = torch.einsum("bhqk,bhkd->bhqd", p, v)
        return (lambda x, *d: x.permute(*d))(o, 0, 2, 1, 3).reshape(B, Sq, Hq * Dh) @ wo

    def _B_ref(x, w_gate, w_up, w_down):
        return (_gelu(x @ w_gate) * (x @ w_up)) @ w_down

    x = A_x
    g = _sigmoid((x * gate_w).sum(-1, keepdims=True))
    a = _A_ref(x, A_k_cache, A_v_cache, A_wq, A_wo)
    b = _B_ref(x, B_w_gate, B_w_up, B_w_down)
    return g * a + (1 - g) * b
```

```cpp
#include <hip/hip_runtime.h>
#include <hip/hip_bf16.h>
#include <stdint.h>

typedef unsigned u32x4 __attribute__((ext_vector_type(4)));
typedef float f32x4 __attribute__((ext_vector_type(4)));
typedef __attribute__((ext_vector_type(2))) __bf16 bf16x2;

constexpr int B = 2, D = 576, HQ = 32, HKV = 16, SK = 32768, DH = 128, F = 4608;
constexpr int NSPLIT = 16;
constexpr int TOK_PER_SPLIT = SK / NSPLIT;
constexpr int WAVES = 4;

__device__ __forceinline__ float bf2f(unsigned short v) { return __uint_as_float(((unsigned)v) << 16); }
__device__ __forceinline__ float dot2(unsigned k, unsigned q, float acc) {
  return __builtin_amdgcn_fdot2_f32_bf16(__builtin_bit_cast(bf16x2, k), __builtin_bit_cast(bf16x2, q), acc, false);
}
template <int CTRL> __device__ __forceinline__ float dpp(float x) {
  return __builtin_bit_cast(float, __builtin_amdgcn_mov_dpp(__builtin_bit_cast(int, x), CTRL, 0xf, 0xf, true));
}
__device__ __forceinline__ unsigned short f2bf(float f) {
  __hip_bfloat16 h = __float2bfloat16(f);
  return __builtin_bit_cast(unsigned short, h);
}

constexpr size_t WS_Q = 0;
constexpr size_t WS_H = 65536;
constexpr size_t WS_PML = 131072;
constexpr size_t WS_PO = 262144;

__global__ void __launch_bounds__(256) k_proj(const unsigned short* __restrict__ x, const unsigned short* __restrict__ wq,
                                              const unsigned short* __restrict__ wg, const unsigned short* __restrict__ wu,
                                              unsigned char* __restrict__ ws) {
  __shared__ float xs[B][D];
  __shared__ float red[2][B][32][64];
  const int tid = threadIdx.x;
  for (int i = tid; i < B * D; i += 256) xs[i / D][i % D] = bf2f(x[i]);
  __syncthreads();
  const int blk = blockIdx.x;
  const bool isq = blk < 64;
  const int cg = tid & 7, rg = tid >> 3;
  const int nmat = isq ? 1 : 2;
  const int col0 = isq ? blk * 64 : (blk - 64) * 64;
  const int ld = isq ? 4096 : F;
  for (int m = 0; m < nmat; ++m) {
    const unsigned short* w = isq ? wq : (m == 0 ? wg : wu);
    float acc[B][8];
#pragma unroll
    for (int b = 0; b < B; ++b)
#pragma unroll
      for (int k = 0; k < 8; ++k) acc[b][k] = 0.f;
#pragma unroll 6
    for (int r = rg; r < D; r += 32) {
      u32x4 v = *reinterpret_cast<const u32x4*>(w + (size_t)r * ld + col0 + cg * 8);
      float x0 = xs[0][r], x1 = xs[1][r];
#pragma unroll
      for (int k = 0; k < 4; ++k) {
        float lo = __uint_as_float(v[k] << 16), hi = __uint_as_float(v[k] & 0xffff0000u);
        acc[0][2 * k] = fmaf(x0, lo, acc[0][2 * k]); acc[0][2 * k + 1] = fmaf(x0, hi, acc[0][2 * k + 1]);
        acc[1][2 * k] = fmaf(x1, lo, acc[1][2 * k]); acc[1][2 * k + 1] = fmaf(x1, hi, acc[1][2 * k + 1]);
      }
    }
#pragma unroll
    for (int b = 0; b < B; ++b)
#pragma unroll
      for (int k = 0; k < 8; ++k) red[m][b][rg][cg * 8 + k] = acc[b][k];
  }
  __syncthreads();
  if (tid < 128) {
    const int b = tid >> 6, c = tid & 63;
    float s0 = 0.f, s1 = 0.f;
    for (int r = 0; r < 32; ++r) { s0 += red[0][b][r][c]; if (!isq) s1 += red[1][b][r][c]; }
    if (isq) {
      reinterpret_cast<unsigned short*>(ws + WS_Q)[b * 4096 + col0 + c] = f2bf(s0);
    } else {
      float g = s0;
      float t = tanhf(0.7978845608028654f * (g + 0.044715f * g * g * g));
      float ge = 0.5f * g * (1.f + t);
      reinterpret_cast<float*>(ws + WS_H)[b * F + col0 + c] = ge * s1;
    }
  }
}

__global__ void __launch_bounds__(256) k_attn(const unsigned short* __restrict__ kc, const unsigned short* __restrict__ vc,
                                              unsigned char* __restrict__ ws) {
  __shared__ float mrg[WAVES * 4][2][DH + 4];
  const int s = blockIdx.x, kvh = blockIdx.y, b = blockIdx.z;
  const int tid = threadIdx.x, lane = tid & 63, wave = __builtin_amdgcn_readfirstlane(tid >> 6), r = lane >> 4, c = lane & 15;
  constexpr float C2 = 0.08838834764831845f * 1.4426950408889634f;
  const unsigned short* q = reinterpret_cast<const unsigned short*>(ws + WS_Q) + b * 4096 + kvh * 256;
  u32x4 q0 = *reinterpret_cast<const u32x4*>(q + 8 * c);
  u32x4 q1 = *reinterpret_cast<const u32x4*>(q + 128 + 8 * c);
  const size_t base = ((size_t)(b * HKV + kvh) * SK + (size_t)s * TOK_PER_SPLIT) * DH;
  const u32x4* kp = reinterpret_cast<const u32x4*>(kc + base) + lane;
  const u32x4* vp = reinterpret_cast<const u32x4*>(vc + base) + lane;
  constexpr int NT = TOK_PER_SPLIT / 16 / WAVES;
  float m = -INFINITY, l = 0.f, acc[2][8];
#pragma unroll
  for (int h = 0; h < 2; ++h)
#pragma unroll
    for (int k = 0; k < 8; ++k) acc[h][k] = 0.f;
  u32x4 Kn[4], Vn[4];
  {
    const int tile = wave;
#pragma unroll
    for (int t = 0; t < 4; ++t) { Kn[t] = __builtin_nontemporal_load(kp + tile * 256 + t * 64); Vn[t] = __builtin_nontemporal_load(vp + tile * 256 + t * 64); }
  }
  for (int i = 0; i < NT; ++i) {
    u32x4 Kc[4], Vc[4];
#pragma unroll
    for (int t = 0; t < 4; ++t) { Kc[t] = Kn[t]; Vc[t] = Vn[t]; }
    if (i + 1 < NT) {
      const int tile = wave + (i + 1) * WAVES;
#pragma unroll
      for (int t = 0; t < 4; ++t) { Kn[t] = __builtin_nontemporal_load(kp + tile * 256 + t * 64); Vn[t] = __builtin_nontemporal_load(vp + tile * 256 + t * 64); }
    }
    float x[8];
#pragma unroll
    for (int t = 0; t < 4; ++t) {
      float d0 = 0.f, d1 = 0.f;
#pragma unroll
      for (int k = 0; k < 4; ++k) { d0 = dot2(Kc[t][k], q0[k], d0); d1 = dot2(Kc[t][k], q1[k], d1); }
      x[t * 2] = d0; x[t * 2 + 1] = d1;
    }
#pragma unroll
    for (int h = 0; h < 4; ++h) { float lo = x[h], hi = x[h + 4]; float t1 = lo + dpp<0x128>(lo), t2 = hi + dpp<0x128>(hi); x[h] = (c & 8) ? t2 : t1; }
#pragma unroll
    for (int h = 0; h < 2; ++h) { float lo = x[h], hi = x[h + 2]; float t1 = lo + dpp<0x141>(lo), t2 = hi + dpp<0x141>(hi); x[h] = (c & 4) ? t2 : t1; }
    { float lo = x[0], hi = x[1]; float t1 = lo + dpp<0x4E>(lo), t2 = hi + dpp<0x4E>(hi); x[0] = (c & 2) ? t2 : t1; }
    float sc = (x[0] + dpp<0xB1>(x[0])) * C2;
    float pm = fmaxf(sc, dpp<0x124>(sc)); pm = fmaxf(pm, dpp<0x128>(pm));
    float mn = fmaxf(m, pm);
    float alpha = __builtin_amdgcn_exp2f(m - mn);
    float p = __builtin_amdgcn_exp2f(sc - mn);
    l = fmaf(l, alpha, p); m = mn;
    float a0 = dpp<0x150 + 0>(alpha), a1 = dpp<0x150 + 2>(alpha);
#pragma unroll
    for (int k = 0; k < 8; ++k) { acc[0][k] *= a0; acc[1][k] *= a1; }
    float pb[4][2];
    pb[0][0] = dpp<0x150 + 0>(p); pb[0][1] = dpp<0x150 + 2>(p);
    pb[1][0] = dpp<0x150 + 4>(p); pb[1][1] = dpp<0x150 + 6>(p);
    pb[2][0] = dpp<0x150 + 8>(p); pb[2][1] = dpp<0x150 + 10>(p);
    pb[3][0] = dpp<0x150 + 12>(p); pb[3][1] = dpp<0x150 + 14>(p);
#pragma unroll
    for (int t = 0; t < 4; ++t) {
#pragma unroll
      for (int k = 0; k < 4; ++k) {
        float lo = __uint_as_float(Vc[t][k] << 16), hi = __uint_as_float(Vc[t][k] & 0xffff0000u);
        acc[0][2 * k] = fmaf(pb[t][0], lo, acc[0][2 * k]); acc[0][2 * k + 1] = fmaf(pb[t][0], hi, acc[0][2 * k + 1]);
        acc[1][2 * k] = fmaf(pb[t][1], lo, acc[1][2 * k]); acc[1][2 * k + 1] = fmaf(pb[t][1], hi, acc[1][2 * k + 1]);
      }
    }
  }
  float lt = l + dpp<0x124>(l); lt = lt + dpp<0x128>(lt);
  const int rec = wave * 4 + r;
#pragma unroll
  for (int h = 0; h < 2; ++h) {
    *reinterpret_cast<f32x4*>(&mrg[rec][h][4 + 8 * c]) = f32x4{acc[h][0], acc[h][1], acc[h][2], acc[h][3]};
    *reinterpret_cast<f32x4*>(&mrg[rec][h][4 + 8 * c + 4]) = f32x4{acc[h][4], acc[h][5], acc[h][6], acc[h][7]};
  }
  if (c == 0) { mrg[rec][0][0] = m; mrg[rec][0][1] = lt; }
  if (c == 2) { mrg[rec][1][0] = m; mrg[rec][1][1] = lt; }
  __syncthreads();
  {
    const int h = tid >> 7, d = tid & 127;
    float M = -INFINITY;
    for (int w = 0; w < WAVES * 4; ++w) M = fmaxf(M, mrg[w][h][0]);
    float L = 0.f, o = 0.f;
    for (int w = 0; w < WAVES * 4; ++w) { float wt = __builtin_amdgcn_exp2f(mrg[w][h][0] - M); L = fmaf(wt, mrg[w][h][1], L); o = fmaf(wt, mrg[w][h][4 + d], o); }
    const size_t slot = ((size_t)(b * HQ + kvh * 2 + h) * NSPLIT + s);
    reinterpret_cast<float*>(ws + WS_PO)[slot * DH + d] = o;
    if (d == 0) { reinterpret_cast<float*>(ws + WS_PML)[slot * 2] = M; reinterpret_cast<float*>(ws + WS_PML)[slot * 2 + 1] = L; }
  }
}

__global__ void __launch_bounds__(1024) k_out(const unsigned short* __restrict__ x, const unsigned short* __restrict__ wo,
                                              const unsigned short* __restrict__ wd, const unsigned short* __restrict__ gw,
                                              const unsigned char* __restrict__ ws, unsigned short* __restrict__ out) {
  __shared__ float ov[B][4096 + F];
  __shared__ float wts[B * HQ][NSPLIT];
  __shared__ float red[16][2][B][8];
  __shared__ float gsh[B];
  const int tid = threadIdx.x;
  const float* pml = reinterpret_cast<const float*>(ws + WS_PML);
  const float* po = reinterpret_cast<const float*>(ws + WS_PO);
  const float* hh = reinterpret_cast<const float*>(ws + WS_H);
  if (tid < B * HQ) {
    float M = -INFINITY;
    for (int s = 0; s < NSPLIT; ++s) M = fmaxf(M, pml[(tid * NSPLIT + s) * 2]);
    float L = 0.f;
    for (int s = 0; s < NSPLIT; ++s) L += __builtin_amdgcn_exp2f(pml[(tid * NSPLIT + s) * 2] - M) * pml[(tid * NSPLIT + s) * 2 + 1];
    for (int s = 0; s < NSPLIT; ++s) wts[tid][s] = __builtin_amdgcn_exp2f(pml[(tid * NSPLIT + s) * 2] - M) / L;
  }
  if (tid >= 128 && tid < 256) {
    const int b = (tid - 128) >> 6, ln = tid & 63;
    float s = 0.f;
    for (int i = ln; i < D; i += 64) s += bf2f(x[b * D + i]) * bf2f(gw[i]);
    for (int o = 32; o > 0; o >>= 1) s += __shfl_xor(s, o);
    if (ln == 0) gsh[b] = 1.f / (1.f + __expf(-s));
  }
  for (int i = tid; i < B * F; i += 1024) ov[i / F][4096 + i % F] = hh[i];
  __syncthreads();
  for (int i = tid; i < B * 4096; i += 1024) {
    const int bh = i >> 7, d = i & 127;
    float o = 0.f;
    for (int s = 0; s < NSPLIT; ++s) o = fmaf(wts[bh][s], po[((size_t)bh * NSPLIT + s) * DH + d], o);
    ov[i >> 12][i & 4095] = o;
  }
  __syncthreads();
  const int col0 = blockIdx.x * 8;
  float acc[2][B][8];
#pragma unroll
  for (int p = 0; p < 2; ++p)
#pragma unroll
    for (int b = 0; b < B; ++b)
#pragma unroll
      for (int k = 0; k < 8; ++k) acc[p][b][k] = 0.f;
  for (int r = tid; r < 4096; r += 1024) {
    u32x4 v = *reinterpret_cast<const u32x4*>(wo + (size_t)r * D + col0);
    float x0 = ov[0][r], x1 = ov[1][r];
#pragma unroll
    for (int k = 0; k < 4; ++k) {
      float lo = __uint_as_float(v[k] << 16), hi = __uint_as_float(v[k] & 0xffff0000u);
      acc[0][0][2 * k] = fmaf(x0, lo, acc[0][0][2 * k]); acc[0][0][2 * k + 1] = fmaf(x0, hi, acc[0][0][2 * k + 1]);
      acc[0][1][2 * k] = fmaf(x1, lo, acc[0][1][2 * k]); acc[0][1][2 * k + 1] = fmaf(x1, hi, acc[0][1][2 * k + 1]);
    }
  }
  for (int r = tid; r < F; r += 1024) {
    u32x4 v = *reinterpret_cast<const u32x4*>(wd + (size_t)r * D + col0);
    float x0 = ov[0][4096 + r], x1 = ov[1][4096 + r];
#pragma unroll
    for (int k = 0; k < 4; ++k) {
      float lo = __uint_as_float(v[k] << 16), hi = __uint_as_float(v[k] & 0xffff0000u);
      acc[1][0][2 * k] = fmaf(x0, lo, acc[1][0][2 * k]); acc[1][0][2 * k + 1] = fmaf(x0, hi, acc[1][0][2 * k + 1]);
      acc[1][1][2 * k] = fmaf(x1, lo, acc[1][1][2 * k]); acc[1][1][2 * k + 1] = fmaf(x1, hi, acc[1][1][2 * k + 1]);
    }
  }
#pragma unroll
  for (int p = 0; p < 2; ++p)
#pragma unroll
    for (int b = 0; b < B; ++b)
#pragma unroll
      for (int k = 0; k < 8; ++k) {
        float s = acc[p][b][k];
        for (int o = 32; o > 0; o >>= 1) s += __shfl_xor(s, o);
        if ((tid & 63) == 0) red[tid >> 6][p][b][k] = s;
      }
  __syncthreads();
  if (tid < 16) {
    const int b = tid >> 3, k = tid & 7;
    float a = 0.f, bb = 0.f;
    for (int w = 0; w < 16; ++w) { a += red[w][0][b][k]; bb += red[w][1][b][k]; }
    const float g = gsh[b];
    out[b * D + col0 + k] = f2bf(g * a + (1.f - g) * bb);
  }
}

extern "C" void kernel_launch(void* const* d_in, const int* in_sizes, int n_in,
                              void* d_out, int out_size, void* d_ws, size_t ws_size,
                              hipStream_t stream) {
  const unsigned short* x = (const unsigned short*)d_in[0];
  const unsigned short* kc = (const unsigned short*)d_in[1];
  const unsigned short* vc = (const unsigned short*)d_in[2];
  const unsigned short* wq = (const unsigned short*)d_in[3];
  const unsigned short* wo = (const unsigned short*)d_in[4];
  const unsigned short* wg = (const unsigned short*)d_in[6];
  const unsigned short* wu = (const unsigned short*)d_in[7];
  const unsigned short* wd = (const unsigned short*)d_in[8];
  const unsigned short* gw = (const unsigned short*)d_in[9];
  unsigned char* ws = (unsigned char*)d_ws;
  k_proj<<<64 + 72, 256, 0, stream>>>(x, wq, wg, wu, ws);
  k_attn<<<dim3(NSPLIT, HKV, B), 256, 0, stream>>>(kc, vc, ws);
  k_out<<<72, 1024, 0, stream>>>(x, wo, wd, gw, ws, (unsigned short*)d_out);
}
```

```cpp
#include <hip/hip_runtime.h>
#include <stdint.h>

typedef unsigned u32x4 __attribute__((ext_vector_type(4)));
typedef unsigned u32x2 __attribute__((ext_vector_type(2)));
typedef float f32x4 __attribute__((ext_vector_type(4)));
typedef __bf16 bf16x2 __attribute__((ext_vector_type(2)));
typedef __bf16 bf16x8 __attribute__((ext_vector_type(8)));
typedef unsigned long long u64;
#define GAS __attribute__((address_space(1)))
#define RLX_AGENT __ATOMIC_RELAXED, __HIP_MEMORY_SCOPE_AGENT

#define KS 26
#define MIMIC 1

constexpr int DM = 576, FF = 4608;
constexpr unsigned GROUP_BYTES = 8u << 20;
constexpr int TILES = 2048;
constexpr int DYN0 = KS * 64;
constexpr unsigned OOB = 0x80000000u;
constexpr unsigned SPIN_CAP = 1u << 16;

constexpr unsigned WS_EPOCH = 0;
constexpr unsigned WS_CTR = 4096;
constexpr unsigned WS_CTRF = WS_CTR + 32 * 256;
constexpr unsigned WS_QP = 32768;
constexpr unsigned WS_FP = WS_QP + 32 * 2 * 8 * 128 * 8;
constexpr unsigned WS_AP = WS_FP + 2 * 3 * 2 * FF * 8;
constexpr unsigned WS_OP = WS_AP + 2 * 32 * 8 * 144 * 8;
constexpr unsigned WS_END = WS_OP + 32 * 2 * DM * 8;
#define STAMP(k) do { } while (0)

__device__ __forceinline__ float dot2(unsigned k, unsigned q, float acc) {
  return __builtin_amdgcn_fdot2_f32_bf16(__builtin_bit_cast(bf16x2, k), __builtin_bit_cast(bf16x2, q), acc, false);
}
template <int CTRL> __device__ __forceinline__ float dpp(float x) {
  return __builtin_bit_cast(float, __builtin_amdgcn_mov_dpp(__builtin_bit_cast(int, x), CTRL, 0xf, 0xf, true));
}
constexpr int XOR1 = 0xB1, XOR2 = 0x4E, XOR7 = 0x141, ROR4 = 0x124, ROR8 = 0x128, ROR12 = 0x12C, IDENT = 0xE4;
__device__ __forceinline__ unsigned cvtpk(float lo, float hi) {
  bf16x2 v = {(__bf16)lo, (__bf16)hi};
  return __builtin_bit_cast(unsigned, v);
}
__device__ __forceinline__ float bf16r(float x) { return __uint_as_float(cvtpk(x, x) << 16); }
__device__ __forceinline__ float xrow_max(float x) {
  auto s = __builtin_amdgcn_permlane16_swap(__float_as_uint(x), __float_as_uint(x), false, false);
  x = fmaxf(__uint_as_float(s[0]), __uint_as_float(s[1]));
  auto t = __builtin_amdgcn_permlane32_swap(__float_as_uint(x), __float_as_uint(x), false, false);
  return fmaxf(__uint_as_float(t[0]), __uint_as_float(t[1]));
}
__device__ __forceinline__ float xrow_sum(float x) {
  auto s = __builtin_amdgcn_permlane16_swap(__float_as_uint(x), __float_as_uint(x), false, false);
  x = __uint_as_float(s[0]) + __uint_as_float(s[1]);
  auto t = __builtin_amdgcn_permlane32_swap(__float_as_uint(x), __float_as_uint(x), false, false);
  return __uint_as_float(t[0]) + __uint_as_float(t[1]);
}
__device__ __forceinline__ f32x4 mfma(u32x4 a, u32x4 b, f32x4 c) {
  return __builtin_amdgcn_mfma_f32_16x16x32_bf16(__builtin_bit_cast(bf16x8, a), __builtin_bit_cast(bf16x8, b), c, 0, 0, 0);
}
__device__ __forceinline__ void put_granule(unsigned char* ws, unsigned off, unsigned tag, float v) {
  __hip_atomic_store((GAS u64*)(ws + off), ((u64)tag << 32) | (u64)__float_as_uint(v), RLX_AGENT);
}
__device__ __forceinline__ void wg_barrier() {
  asm volatile("s_waitcnt lgkmcnt(0)" ::: "memory");
  __builtin_amdgcn_s_barrier();
  asm volatile("" ::: "memory");
}
#define RSRC(p, n) __builtin_amdgcn_make_buffer_rsrc((void*)(p), 0, (n), 0x00020000)
#define LD128(r, off, aux) __builtin_amdgcn_raw_buffer_load_b128((r), (off), 0, (aux))
#define LD64(r, off, aux) __builtin_amdgcn_raw_buffer_load_b64((r), (off), 0, (aux))
#define LD32(r, off, aux) __builtin_amdgcn_raw_buffer_load_b32((r), (off), 0, (aux))
#define LD16(r, off, aux) __builtin_amdgcn_raw_buffer_load_b16((r), (off), 0, (aux))
constexpr int NT_ = 2, SC1 = 16;

__device__ __forceinline__ int dimg(int sel, int n) {
  const int chunk = n >> 3, jj = n & 7;
  return ((chunk >> 2) * 16 + 4 * (jj >> 1) + 2 * sel + (jj & 1)) * 4 + (chunk & 3);
}

__global__ void __launch_bounds__(512, 2)
fused_decode(const unsigned short* __restrict__ x, const unsigned short* __restrict__ kc, const unsigned short* __restrict__ vc,
             const unsigned short* __restrict__ wq, const unsigned short* __restrict__ wo, const unsigned short* __restrict__ wg,
             const unsigned short* __restrict__ wu, const unsigned short* __restrict__ wd, const unsigned short* __restrict__ gw,
             unsigned short* __restrict__ out, unsigned char* ws) {
  __shared__ __attribute__((aligned(16))) float lds[4 * 2048 + 128 + 32];
  float* red_q = lds;
  float* red_f = lds + 2048;
  float* mrg = lds + 4096;
  float* red_o = lds + 6144;
  unsigned* qbuf = (unsigned*)(lds + 8192);
  float* ml = lds + 8192 + 128;

  const int bid = blockIdx.x, tid = threadIdx.x, lane = tid & 63;
  const int wave = __builtin_amdgcn_readfirstlane(tid >> 6);
  const int g = lane >> 4, i = lane & 15;
  const int sel = (i >> 1) & 1;
  const int grp = bid >> 3, mem = bid & 7;
  const int b = grp >> 4, kvh = grp & 15;
  STAMP(0);

  const __amdgpu_buffer_rsrc_t xr = RSRC(x, 2 * DM * 2);
  const __amdgpu_buffer_rsrc_t wsr = RSRC(ws, WS_END);
  const unsigned epoch = LD32(wsr, WS_EPOCH, 0);
  __builtin_amdgcn_sched_barrier(0);
  const __amdgpu_buffer_rsrc_t kr = RSRC((const char*)kc + (size_t)grp * GROUP_BYTES, GROUP_BYTES);
  const __amdgpu_buffer_rsrc_t vr = RSRC((const char*)vc + (size_t)grp * GROUP_BYTES, GROUP_BYTES);

  const bool has_f = mem < 6 || (mem == 6 && (grp & 3) != 3);
  const int u = mem < 6 ? grp * 6 + mem : 192 + grp - (grp >> 2);
  const int fmat = u >= 108 ? 1 : 0, fcb = (u - 108 * fmat) / 3, fkt = (u - 108 * fmat) % 3;

  unsigned xq[3], xf[6];
  u32x4 aq[3], af[6];
  {
    const __amdgpu_buffer_rsrc_t wqr = RSRC(wq, DM * 4096 * 2);
    const __amdgpu_buffer_rsrc_t wfr = RSRC(fmat ? wu : wg, has_f ? DM * FF * 2 : 0);
#pragma unroll
    for (int k = 0; k < 3; ++k) {
      const int st = wave + 8 * k, row = 72 * mem + 4 * st + g;
      xq[k] = LD16(xr, st < 18 ? (unsigned)(sel * DM + row) * 2u : OOB, 0);
    }
#pragma unroll
    for (int k = 0; k < 3; ++k) {
      const int st = wave + 8 * k, row = 72 * mem + 4 * st + g;
      aq[k] = LD128(wqr, st < 18 ? (unsigned)row * 8192u + (unsigned)grp * 256u + 16u * i : OOB, NT_);
    }
    __builtin_amdgcn_sched_barrier(0);
#pragma unroll
    for (int k = 0; k < 6; ++k) {
      const int row = 192 * fkt + 4 * (wave + 8 * k) + g;
      xf[k] = LD16(xr, (unsigned)(sel * DM + row) * 2u, 0);
    }
#pragma unroll
    for (int k = 0; k < 6; ++k) {
      const int row = 192 * fkt + 4 * (wave + 8 * k) + g;
      af[k] = LD128(wfr, (unsigned)row * (FF * 2u) + (unsigned)fcb * 256u + 16u * i, NT_);
    }
    __builtin_amdgcn_sched_barrier(0);
  }
  unsigned gx[9], gg[9];
  {
    const __amdgpu_buffer_rsrc_t gwr = RSRC(gw, DM * 2);
#pragma unroll
    for (int k = 0; k < 9; ++k) {
      const unsigned dw = (unsigned)(lane & 31) + 32u * k;
      gx[k] = LD32(xr, (unsigned)(lane >> 5) * (DM * 2u) + dw * 4u, 0);
      gg[k] = LD32(gwr, dw * 4u, 0);
    }
  }
  __builtin_amdgcn_sched_barrier(0);

  u32x4 ka[4], va[4], kb[4], vb[4];
  const unsigned lo = lane * 16;
  const int sb = mem * 8 + wave;
#define LOADT(kk, vv, t) do { const unsigned off_ = lo + (unsigned)(t) * 4096u; \
    _Pragma("unroll") for (int j_ = 0; j_ < 4; ++j_) kk[j_] = LD128(kr, off_ + j_ * 1024, NT_); \
    _Pragma("unroll") for (int j_ = 0; j_ < 4; ++j_) vv[j_] = LD128(vr, off_ + j_ * 1024, NT_); \
    __builtin_amdgcn_sched_barrier(0); } while (0)
#define WAITT(kk, vv) do { _Pragma("unroll") for (int j_ = 0; j_ < 4; ++j_) { asm volatile("" : "+v"(kk[j_]), "+v"(vv[j_])); } \
    __builtin_amdgcn_sched_barrier(0); } while (0)
  LOADT(ka, va, sb);

  const unsigned hmask = (i & 1) ? 0xffff0000u : 0x0000ffffu;
  u32x4 msk;
#pragma unroll
  for (int m = 0; m < 4; ++m) msk[m] = ((i >> 2) == m) ? hmask : 0u;
#define BFRAG(dup) (u32x4{(dup) & msk[0], (dup) & msk[1], (dup) & msk[2], (dup) & msk[3]})

  const unsigned tag = epoch + 0x9E3779B9u;
  {
    f32x4 d = {0.f, 0.f, 0.f, 0.f};
#pragma unroll
    for (int k = 0; k < 3; ++k) { const unsigned xv = xq[k] & 0xffffu; d = mfma(aq[k], BFRAG(xv | (xv << 16)), d); }
    *(f32x4*)(red_q + wave * 256 + lane * 4) = d;
  }
  wg_barrier();
  if (tid < 256) {
    const int bb = tid >> 7, n = tid & 127, idx = dimg(bb, n);
    float s = 0.f;
#pragma unroll
    for (int w = 0; w < 8; ++w) s += red_q[w * 256 + idx];
    put_granule(ws, WS_QP + (unsigned)(((grp * 2 + bb) * 8 + mem) * 128 + n) * 8u, tag, s);
  }
  STAMP(1);
  {
    f32x4 d = {0.f, 0.f, 0.f, 0.f};
#pragma unroll
    for (int k = 0; k < 6; ++k) { const unsigned xv = xf[k] & 0xffffu; d = mfma(af[k], BFRAG(xv | (xv << 16)), d); }
    *(f32x4*)(red_f + wave * 256 + lane * 4) = d;
  }
  wg_barrier();
  if (has_f && tid >= 256) {
    const int bb = (tid >> 7) & 1, n = tid & 127, idx = dimg(bb, n);
    float s = 0.f;
#pragma unroll
    for (int w = 0; w < 8; ++w) s += red_f[w * 256 + idx];
    put_granule(ws, WS_FP + (unsigned)(((fmat * 3 + fkt) * 2 + bb) * FF + fcb * 128 + n) * 8u, tag, s);
  }
  float gate;
  {
    float s = 0.f;
#pragma unroll
    for (int k = 0; k < 9; ++k) {
      const float p0 = __uint_as_float(gx[k] << 16) * __uint_as_float(gg[k] << 16);
      const float p1 = __uint_as_float(gx[k] & 0xffff0000u) * __uint_as_float(gg[k] & 0xffff0000u);
      s += MIMIC ? bf16r(p0) + bf16r(p1) : p0 + p1;
    }
    s += dpp<XOR1>(s); s += dpp<XOR2>(s); s += dpp<ROR4>(s); s += dpp<ROR8>(s);
    auto t = __builtin_amdgcn_permlane16_swap(__float_as_uint(s), __float_as_uint(s), false, false);
    s = __uint_as_float(t[0]) + __uint_as_float(t[1]);
    const float s0 = __builtin_bit_cast(float, __builtin_amdgcn_readlane(__builtin_bit_cast(int, s), 0));
    const float s1 = __builtin_bit_cast(float, __builtin_amdgcn_readlane(__builtin_bit_cast(int, s), 32));
    float z = sel ? s1 : s0;
    if (MIMIC) z = bf16r(z);
    gate = 1.f / (1.f + __expf(-z));
    if (MIMIC) gate = bf16r(gate);
  }
  STAMP(2);
  u32x4 q0, q1;
  {
    const int s = lane & 7, eq = 32 * wave + 4 * (lane >> 3);
    const unsigned off = WS_QP + (unsigned)((((2 * kvh + (eq >> 7)) * 2 + b) * 8 + s) * 128 + (eq & 127)) * 8u;
    asm volatile("" ::: "memory");
    u32x4 a0 = LD128(wsr, off, SC1), a1 = LD128(wsr, off + 16, SC1);
    __builtin_amdgcn_sched_barrier(0);
    LOADT(kb, vb, sb + 64);
    if (!__all(a0[1] == tag && a0[3] == tag && a1[1] == tag && a1[3] == tag)) {
      unsigned spins = 0;
      do {
        __builtin_amdgcn_s_sleep(4);
        asm volatile("" ::: "memory");
        a0 = LD128(wsr, off, SC1); a1 = LD128(wsr, off + 16, SC1);
      } while (!__all(a0[1] == tag && a0[3] == tag && a1[1] == tag && a1[3] == tag) && ++spins < SPIN_CAP);
    }
    float v[4] = {__uint_as_float(a0[0]), __uint_as_float(a0[2]), __uint_as_float(a1[0]), __uint_as_float(a1[2])};
#pragma unroll
    for (int k = 0; k < 4; ++k) { v[k] += dpp<XOR1>(v[k]); v[k] += dpp<XOR2>(v[k]); v[k] += dpp<XOR7>(v[k]); }
    if (s == 0) *(u32x2*)(qbuf + (eq >> 1)) = u32x2{cvtpk(v[0], v[1]), cvtpk(v[2], v[3])};
    wg_barrier();
    q0 = *(const u32x4*)(qbuf + 4 * i);
    q1 = *(const u32x4*)(qbuf + 64 + 4 * i);
  }
  STAMP(3);

  constexpr float SCALE = 0.08838834764831845f, LOG2E = 1.4426950408889634f;
  float m = -INFINITY, l = 0.f;
  f32x4 D = {0.f, 0.f, 0.f, 0.f};
  u32x4 pf = {0u, 0u, 0u, 0u};
#define PSLOT(s_, vreg) do { \
    pf[(s_) & 3] = __builtin_amdgcn_update_dpp(pf[(s_) & 3], pq, IDENT, 0xf, 1 << ((s_) & 3), false); \
    pf[((s_) + 1) & 3] = __builtin_amdgcn_update_dpp(pf[((s_) + 1) & 3], pq, ROR4, 0xf, 1 << (((s_) + 1) & 3), false); \
    pf[((s_) + 2) & 3] = __builtin_amdgcn_update_dpp(pf[((s_) + 2) & 3], pq, ROR8, 0xf, 1 << (((s_) + 2) & 3), false); \
    pf[((s_) + 3) & 3] = __builtin_amdgcn_update_dpp(pf[((s_) + 3) & 3], pq, ROR12, 0xf, 1 << (((s_) + 3) & 3), false); \
    D = mfma(vreg, pf, D); } while (0)
#define COMP(kk, vv) do { \
    float xx[8]; \
    _Pragma("unroll") for (int t_ = 0; t_ < 4; ++t_) { float d0 = 0.f, d1 = 0.f; \
      _Pragma("unroll") for (int k_ = 0; k_ < 4; ++k_) { d0 = dot2(kk[t_][k_], q0[k_], d0); d1 = dot2(kk[t_][k_], q1[k_], d1); } \
      xx[t_ * 2] = d0; xx[t_ * 2 + 1] = d1; } \
    _Pragma("unroll") for (int h_ = 0; h_ < 4; ++h_) { const float lo_ = xx[h_], hi_ = xx[h_ + 4]; const float t1 = lo_ + dpp<ROR8>(lo_), t2 = hi_ + dpp<ROR8>(hi_); xx[h_] = (i & 8) ? t2 : t1; } \
    _Pragma("unroll") for (int h_ = 0; h_ < 2; ++h_) { const float lo_ = xx[h_], hi_ = xx[h_ + 2]; const float t1 = lo_ + dpp<XOR7>(lo_), t2 = hi_ + dpp<XOR7>(hi_); xx[h_] = (i & 4) ? t2 : t1; } \
    { const float lo_ = xx[0], hi_ = xx[1]; const float t1 = lo_ + dpp<XOR2>(lo_), t2 = hi_ + dpp<XOR2>(hi_); xx[0] = (i & 2) ? t2 : t1; } \
    float sc = xx[0] + dpp<XOR1>(xx[0]); \
    if (MIMIC) sc = bf16r(bf16r(sc) * SCALE) * LOG2E; else sc *= (SCALE * LOG2E); \
    float pm = fmaxf(sc, dpp<ROR4>(sc)); pm = fmaxf(pm, dpp<ROR8>(pm)); pm = xrow_max(pm); \
    const float mn = fmaxf(m, pm); \
    const float alpha = __builtin_amdgcn_exp2f(m - mn), p = __builtin_amdgcn_exp2f(sc - mn); \
    l = fmaf(l, alpha, p); m = mn; D *= alpha; \
    const unsigned pq = cvtpk(p, p) & hmask; \
    PSLOT(0, vv[0]); PSLOT(1, vv[1]); PSLOT(2, vv[2]); PSLOT(3, vv[3]); \
    asm volatile("" : "+v"(D), "+v"(l), "+v"(m)); \
    __builtin_amdgcn_sched_barrier(0); } while (0)

  GAS unsigned* ctr = (GAS unsigned*)(ws + WS_CTR + grp * 256);
  const bool ctr_owner = (mem == 0 && wave == 0);
  unsigned zoff = 0; asm volatile("" : "+v"(zoff));
  const int hidx = grp, jn = mem;
  u32x4 ao[4], ad[5];
  u32x2 fpv[5];
  unsigned fpo[5];
  if (ctr_owner && lane == 0) __hip_atomic_store(ctr, 0u, RLX_AGENT);
  COMP(ka, va);
  unsigned cl = 0;
  u32x2 cf = {0u, 0u};
  for (int k = 1;; ++k) {
    WAITT(kb, vb);
#pragma unroll
    for (int j = 0; j < 4; ++j) { ka[j] = kb[j]; va[j] = vb[j]; }
    WAITT(ka, va);
    if (k == 1 && ctr_owner) {
      asm volatile("s_waitcnt vmcnt(0)" ::: "memory");
      if (lane == 0) put_granule(ws, WS_CTRF + grp * 256, tag, 0.f);
    }
    if (k == KS - 3) cf = LD64(wsr, WS_CTRF + grp * 256, SC1);
    if (k == KS - 2) {
      STAMP(4);
      if (!__all(cf[1] == tag)) {
        unsigned spins = 0;
        do {
          __builtin_amdgcn_s_sleep(4);
          asm volatile("" ::: "memory");
          cf = LD64(wsr, WS_CTRF + grp * 256, SC1);
        } while (!__all(cf[1] == tag) && ++spins < SPIN_CAP);
      }
      asm volatile("" ::: "memory");
    }
    const unsigned id = (k + 1 < KS) ? (unsigned)(sb + (k + 1) * 64) : DYN0 + (unsigned)__builtin_amdgcn_readfirstlane(cl);
    if (id >= (unsigned)TILES) break;
    if (k + 2 >= KS && lane == 0) cl = __hip_atomic_fetch_add(ctr + zoff, 1u, RLX_AGENT);
    __builtin_amdgcn_sched_barrier(0);
    LOADT(kb, vb, id);
    COMP(ka, va);
  }
  {
    const __amdgpu_buffer_rsrc_t wor = RSRC(wo, 4096 * DM * 2);
    const __amdgpu_buffer_rsrc_t wdr = RSRC(wd, FF * DM * 2);
#pragma unroll
    for (int st = 0; st < 4; ++st) {
      const int row = 128 * hidx + 16 * wave + 4 * g + st;
      ao[st] = LD128(wor, i < 9 ? (unsigned)row * (DM * 2u) + 144u * jn + 16u * i : OOB, NT_);
    }
#pragma unroll
    for (int st = 0; st < 5; ++st) {
      const int rl = 4 * st + g, row = 144 * hidx + 18 * wave + rl;
      ad[st] = LD128(wdr, (i < 9 && rl < 18) ? (unsigned)row * (DM * 2u) + 144u * jn + 16u * i : OOB, NT_);
      fpo[st] = (rl < 18 && (i >> 2) < 3) ? WS_FP + (unsigned)((((i & 1) * 3 + (i >> 2)) * 2 + sel) * FF + row) * 8u : OOB;
      fpv[st] = LD64(wsr, fpo[st], SC1);
    }
    __builtin_amdgcn_sched_barrier(0);
  }
  COMP(ka, va);
  STAMP(5);

  {
    float lt = l + dpp<ROR4>(l); lt += dpp<ROR8>(lt); lt = xrow_sum(lt);
    *(f32x4*)(mrg + wave * 256 + lane * 4) = D;
    if (lane == 0 || lane == 2) { ml[(wave * 2 + sel) * 2] = m; ml[(wave * 2 + sel) * 2 + 1] = lt; }
  }
  wg_barrier();
  STAMP(6);
  if (tid < 320) {
    const int h = tid < 256 ? (tid >> 7) : (tid & 1), n = tid & 127, idx = dimg(h, n);
    float M = -INFINITY;
#pragma unroll
    for (int w = 0; w < 8; ++w) M = fmaxf(M, ml[(w * 2 + h) * 2]);
    const unsigned base = WS_AP + (unsigned)(((b * 32 + 2 * kvh + h) * 8 + mem) * 144) * 8u;
    if (tid < 256) {
      float o = 0.f;
#pragma unroll
      for (int w = 0; w < 8; ++w) o = fmaf(__builtin_amdgcn_exp2f(ml[(w * 2 + h) * 2] - M), mrg[w * 256 + idx], o);
      put_granule(ws, base + n * 8u, tag, o);
    } else if (tid < 258) {
      float L = 0.f;
#pragma unroll
      for (int w = 0; w < 8; ++w) L = fmaf(__builtin_amdgcn_exp2f(ml[(w * 2 + h) * 2] - M), ml[(w * 2 + h) * 2 + 1], L);
      put_granule(ws, base + 128 * 8u, tag, M);
      put_granule(ws, base + 129 * 8u, tag, L);
    }
  }
  STAMP(7);
  const int sp = (i & 1) | ((i >> 2) << 1);
  const unsigned abase = WS_AP + (unsigned)(((sel * 32 + hidx) * 8 + sp) * 144) * 8u;
  const unsigned aoff = abase + (unsigned)(16 * wave + 4 * g) * 8u;
  asm volatile("" ::: "memory");
  u32x4 a0 = LD128(wsr, aoff, SC1), a1 = LD128(wsr, aoff + 16, SC1), am = LD128(wsr, abase + 128 * 8u, SC1);
  __builtin_amdgcn_sched_barrier(0);

  f32x4 Df = {0.f, 0.f, 0.f, 0.f};
  {
#define FP_OK() ((fpo[0] == OOB || fpv[0][1] == tag) && (fpo[1] == OOB || fpv[1][1] == tag) && (fpo[2] == OOB || fpv[2][1] == tag) && \
                 (fpo[3] == OOB || fpv[3][1] == tag) && (fpo[4] == OOB || fpv[4][1] == tag))
    if (!__all(FP_OK())) {
      unsigned spins = 0;
      do {
        __builtin_amdgcn_s_sleep(4);
        asm volatile("" ::: "memory");
#pragma unroll
        for (int st = 0; st < 5; ++st) fpv[st] = LD64(wsr, fpo[st], SC1);
      } while (!__all(FP_OK()) && ++spins < SPIN_CAP);
    }
#pragma unroll
    for (int st = 0; st < 5; ++st) {
      float s = __uint_as_float(fpv[st][0]);
      s += dpp<ROR4>(s); s += dpp<ROR8>(s);
      const float o = dpp<XOR1>(s);
      float gv = (i & 1) ? o : s, uv = (i & 1) ? s : o;
      if (MIMIC) { gv = bf16r(gv); uv = bf16r(uv); }
      const float z2 = 1.5957691216057308f * (gv + 0.044715f * gv * gv * gv);
      float ge = gv / (1.f + __expf(-z2));
      if (MIMIC) ge = bf16r(ge);
      const float hv = ge * uv;
      Df = mfma(ad[st], BFRAG(cvtpk(hv, hv)), Df);
    }
  }
  STAMP(8);
  f32x4 Da = {0.f, 0.f, 0.f, 0.f};
  {
#define A_OK() (a0[1] == tag && a0[3] == tag && a1[1] == tag && a1[3] == tag && am[1] == tag && am[3] == tag)
    bool ok = A_OK();
    if (!__all(ok)) {
      unsigned spins = 0;
      do {
        __builtin_amdgcn_s_sleep(2);
        asm volatile("" ::: "memory");
        if (!ok) { a0 = LD128(wsr, aoff, SC1); a1 = LD128(wsr, aoff + 16, SC1); am = LD128(wsr, abase + 128 * 8u, SC1); }
        ok = A_OK();
      } while (!__all(ok) && ++spins < SPIN_CAP);
    }
    STAMP(9);
    const float ms = __uint_as_float(am[0]), ls = __uint_as_float(am[2]);
    float M = fmaxf(ms, dpp<XOR1>(ms)); M = fmaxf(M, dpp<ROR4>(M)); M = fmaxf(M, dpp<ROR8>(M));
    const float wt = __builtin_amdgcn_exp2f(ms - M);
    float L = wt * ls; L += dpp<XOR1>(L); L += dpp<ROR4>(L); L += dpp<ROR8>(L);
    const float sc = wt / L;
    float o[4] = {__uint_as_float(a0[0]) * sc, __uint_as_float(a0[2]) * sc, __uint_as_float(a1[0]) * sc, __uint_as_float(a1[2]) * sc};
#pragma unroll
    for (int st = 0; st < 4; ++st) {
      o[st] += dpp<XOR1>(o[st]); o[st] += dpp<ROR4>(o[st]); o[st] += dpp<ROR8>(o[st]);
      Da = mfma(ao[st], BFRAG(cvtpk(o[st], o[st])), Da);
    }
  }
  {
    const float g1 = MIMIC ? bf16r(1.f - gate) : 1.f - gate;
    *(f32x4*)(red_o + wave * 256 + lane * 4) = Da * gate + Df * g1;
  }
  wg_barrier();
  if (tid < 144) {
    const int bb = tid >= 72 ? 1 : 0, n = tid - 72 * bb, idx = dimg(bb, n);
    float s = 0.f;
#pragma unroll
    for (int w = 0; w < 8; ++w) s += red_o[w * 256 + idx];
    put_granule(ws, WS_OP + (unsigned)((hidx * 2 + bb) * DM + 72 * jn + n) * 8u, tag, s);
  }
  STAMP(10);
  if (wave == 7 && bid < 144) {
    const int bb = bid >= 72 ? 1 : 0, cb = bid - 72 * bb;
    const unsigned off = WS_OP + (unsigned)(((lane >> 1) * 2 + bb) * DM + 8 * cb + 4 * (lane & 1)) * 8u;
    u32x4 r0 = LD128(wsr, off, SC1), r1 = LD128(wsr, off + 16, SC1);
#define R_OK() (r0[1] == tag && r0[3] == tag && r1[1] == tag && r1[3] == tag)
    bool ok = R_OK();
    if (!__all(ok)) {
      unsigned spins = 0;
      do {
        __builtin_amdgcn_s_sleep(2);
        asm volatile("" ::: "memory");
        if (!ok) { r0 = LD128(wsr, off, SC1); r1 = LD128(wsr, off + 16, SC1); }
        ok = R_OK();
      } while (!__all(ok) && ++spins < SPIN_CAP);
    }
    float v[4] = {__uint_as_float(r0[0]), __uint_as_float(r0[2]), __uint_as_float(r1[0]), __uint_as_float(r1[2])};
#pragma unroll
    for (int k = 0; k < 4; ++k) { v[k] += dpp<XOR2>(v[k]); v[k] += dpp<ROR4>(v[k]); v[k] += dpp<ROR8>(v[k]); v[k] = xrow_sum(v[k]); }
    if (lane < 2) *(u32x2*)(out + bb * DM + 8 * cb + 4 * lane) = u32x2{cvtpk(v[0], v[1]), cvtpk(v[2], v[3])};
    if (bid == 0 && lane == 0) __hip_atomic_store((GAS unsigned*)(ws + WS_EPOCH), tag, RLX_AGENT);
    STAMP(11);
  }
}

extern "C" void kernel_launch(void* const* d_in, const int* in_sizes, int n_in,
                              void* d_out, int out_size, void* d_ws, size_t ws_size,
                              hipStream_t stream) {
  fused_decode<<<256, 512, 0, stream>>>((const unsigned short*)d_in[0], (const unsigned short*)d_in[1], (const unsigned short*)d_in[2],
                                        (const unsigned short*)d_in[3], (const unsigned short*)d_in[4], (const unsigned short*)d_in[6],
                                        (const unsigned short*)d_in[7], (const unsigned short*)d_in[8], (const unsigned short*)d_in[9],
                                        (unsigned short*)d_out, (unsigned char*)d_ws);
}
```

```cpp
#include <hip/hip_runtime.h>
#include <stdint.h>

typedef unsigned u32x4 __attribute__((ext_vector_type(4)));
typedef unsigned u32x2 __attribute__((ext_vector_type(2)));
typedef float f32x4 __attribute__((ext_vector_type(4)));
typedef __bf16 bf16x2 __attribute__((ext_vector_type(2)));
typedef __bf16 bf16x8 __attribute__((ext_vector_type(8)));
typedef unsigned long long u64;
#define GAS __attribute__((address_space(1)))
#define LAS __attribute__((address_space(3)))
#define RLX_AGENT __ATOMIC_RELAXED, __HIP_MEMORY_SCOPE_AGENT
#define RLX_WG __ATOMIC_RELAXED, __HIP_MEMORY_SCOPE_WORKGROUP

#define NWIN 6
#define PWIN 6
#define FWIN 16
#define WWIN 8
#define PACING 1
#define PTAU 14u
#define PSLEEP2 100
#define PSLEEP 100
#define NEAR 8
#define PBASE 1
#define PT1 40
#define PT2 100000
#define HT1 16
#define HT2 36
#define HT3 112
#define FEW 2
#define MIMIC 1

constexpr int DM = 576, FF = 4608;
constexpr unsigned OOB = 0x80000000u;
constexpr unsigned SPIN_CAP = 1u << 16;
constexpr int NTILE = 64;

constexpr unsigned WS_EPOCH = 0;
constexpr unsigned WS_QP = 4096;
constexpr unsigned WS_FP = WS_QP + 32 * 16 * 1024;
constexpr unsigned WS_AP = WS_FP + 36 * 12 * 1024;
constexpr unsigned WS_ML = WS_AP + 32 * 16384;
constexpr unsigned WS_OP = WS_ML + 32 * 16 * 128;
constexpr unsigned WS_HP = WS_OP + 80 * 4096;
constexpr unsigned WS_PG = WS_HP + FF * 8;
constexpr unsigned WS_END = WS_PG + 256 * 128;
#define STAMP(k) do { } while (0)

__device__ __forceinline__ float dot2(unsigned k, unsigned q, float acc) {
  return __builtin_amdgcn_fdot2_f32_bf16(__builtin_bit_cast(bf16x2, k), __builtin_bit_cast(bf16x2, q), acc, false);
}
template <int CTRL> __device__ __forceinline__ float dpp(float x) {
  return __builtin_bit_cast(float, __builtin_amdgcn_mov_dpp(__builtin_bit_cast(int, x), CTRL, 0xf, 0xf, true));
}
constexpr int XOR1 = 0xB1, XOR2 = 0x4E, XOR7 = 0x141, ROR4 = 0x124, ROR8 = 0x128, ROR12 = 0x12C, IDENT = 0xE4;
__device__ __forceinline__ unsigned cvtpk(float lo, float hi) {
  bf16x2 v = {(__bf16)lo, (__bf16)hi};
  return __builtin_bit_cast(unsigned, v);
}
__device__ __forceinline__ float bf16r(float x) { return __uint_as_float(cvtpk(x, x) << 16); }
__device__ __forceinline__ float xrow_max(float x) {
  auto s = __builtin_amdgcn_permlane16_swap(__float_as_uint(x), __float_as_uint(x), false, false);
  x = fmaxf(__uint_as_float(s[0]), __uint_as_float(s[1]));
  auto t = __builtin_amdgcn_permlane32_swap(__float_as_uint(x), __float_as_uint(x), false, false);
  return fmaxf(__uint_as_float(t[0]), __uint_as_float(t[1]));
}
__device__ __forceinline__ float xrow_sum(float x) {
  auto s = __builtin_amdgcn_permlane16_swap(__float_as_uint(x), __float_as_uint(x), false, false);
  x = __uint_as_float(s[0]) + __uint_as_float(s[1]);
  auto t = __builtin_amdgcn_permlane32_swap(__float_as_uint(x), __float_as_uint(x), false, false);
  return __uint_as_float(t[0]) + __uint_as_float(t[1]);
}
__device__ __forceinline__ f32x4 mfma(u32x4 a, u32x4 b, f32x4 c) {
  return __builtin_amdgcn_mfma_f32_16x16x32_bf16(__builtin_bit_cast(bf16x8, a), __builtin_bit_cast(bf16x8, b), c, 0, 0, 0);
}
__device__ __forceinline__ void put_granule(unsigned char* ws, unsigned off, unsigned tag, float v) {
  __hip_atomic_store((GAS u64*)(ws + off), ((u64)tag << 32) | (u64)__float_as_uint(v), RLX_AGENT);
}
__device__ __forceinline__ unsigned duplo(unsigned x) { return __builtin_amdgcn_perm(x, x, 0x01000100u); }
__device__ __forceinline__ unsigned duphi(unsigned x) { return __builtin_amdgcn_perm(x, x, 0x03020302u); }
__device__ __forceinline__ unsigned lds_arrive(LAS unsigned* c, int lane) {
  asm volatile("s_waitcnt lgkmcnt(0)" ::: "memory");
  unsigned r = 0;
  if (lane == 0) r = __hip_atomic_fetch_add(c, 1u, RLX_WG);
  r = __builtin_amdgcn_readfirstlane(r);
  asm volatile("" ::: "memory");
  return r;
}
template <int SLEEP> __device__ __forceinline__ void lds_wait(LAS unsigned* c, unsigned want) {
#pragma nounroll
  for (unsigned spins = 0; spins < (1u << 22); ++spins) {
    if (__builtin_amdgcn_readfirstlane(__hip_atomic_load(c, RLX_WG)) >= want) break;
    __builtin_amdgcn_s_sleep(SLEEP);
  }
  asm volatile("" ::: "memory");
}
#define RSRC(p, n) __builtin_amdgcn_make_buffer_rsrc((void*)(p), 0, (n), 0x00020000)
#define LD128(r, off, aux) __builtin_amdgcn_raw_buffer_load_b128((r), (off), 0, (aux))
#define LD64(r, off, aux) __builtin_amdgcn_raw_buffer_load_b64((r), (off), 0, (aux))
#define LD32(r, off, aux) __builtin_amdgcn_raw_buffer_load_b32((r), (off), 0, (aux))
#define SCHED() __builtin_amdgcn_sched_barrier(0)
#define ST2(off, va, vb) __builtin_amdgcn_raw_buffer_store_b128(u32x4{__float_as_uint(va), tag, __float_as_uint(vb), tag}, wsr, (off), 0, 16)
constexpr int NT_ = 2, SC1 = 16;

__global__ void __launch_bounds__(512, 2)
fused_decode(const unsigned short* __restrict__ wq, const unsigned short* __restrict__ x, unsigned char* ws,
             const unsigned short* __restrict__ kc, const unsigned short* __restrict__ vc,
             const unsigned short* __restrict__ wg, const unsigned short* __restrict__ wu, const unsigned short* __restrict__ gw,
             const unsigned short* __restrict__ wo, const unsigned short* __restrict__ wd, unsigned short* __restrict__ out) {
  __shared__ __attribute__((aligned(16))) float lds[3 * 1024 + 128 + 512 + 16 + 4 * 4096 + 256];
  float* sbuf = lds + 3 * 1024 + 128 + 512 + 16;
  LAS unsigned* prog = (LAS unsigned*)(lds + 3 * 1024 + 128 + 512 + 16 + 4 * 4096);
  float* red_f = lds;
  float* mrg = lds + 1024;
  float* red_o = lds + 2048;
  unsigned* qbuf = (unsigned*)(lds + 3072);
  float* ml = lds + 3072 + 128;
  LAS unsigned* flg = (LAS unsigned*)(lds + 3072 + 128 + 512);

  const int bid = blockIdx.x, tid = threadIdx.x, lane = tid & 63;
  const int wave = __builtin_amdgcn_readfirstlane(tid >> 6);
  const int g = lane >> 4, i = lane & 15;
  const int sel = (i >> 1) & 1;
  const int jj = 2 * (i >> 2) + (i & 1);
  const int grp = bid >> 3, mem = bid & 7;
  asm volatile("" :: "s"(wq), "s"(x), "s"(ws), "s"(kc), "s"(vc), "s"(wg), "s"(wu), "s"(gw), "s"(wo), "s"(wd), "s"(out));
  STAMP(0);

  const __amdgpu_buffer_rsrc_t wsr = RSRC(ws, WS_END);
  const unsigned lo = lane * 16;
  const unsigned hmask = (i & 1) ? 0xffff0000u : 0x0000ffffu;

  if (wave < 4) {
    const __amdgpu_buffer_rsrc_t kr = RSRC((const char*)kc + ((size_t)bid << 20), 1u << 20);
    const __amdgpu_buffer_rsrc_t vr = RSRC((const char*)vc + ((size_t)bid << 20), 1u << 20);
    __builtin_amdgcn_s_barrier();
    SCHED();
    u32x4 r[32];
    const unsigned off = lo + (unsigned)wave * 4096u;
#define LDR(j, rs, o) (r[j] = LD128(rs, (o) + ((j) >> 2) * 16384u + ((j) & 3) * 1024u, NT_))
#pragma unroll
    for (int j = 0; j < 32; ++j) {
      if (j >= PWIN) { asm volatile("s_waitcnt vmcnt(%0)" :: "n"(PWIN - 1) : "memory"); }
      SCHED();
      LDR(j, kr, off);
      SCHED();
    }
    STAMP(13);
    lds_wait<1>(flg + 0, 1u);
    const u32x4 q0 = *(const u32x4*)(qbuf + 4 * i);
    const u32x4 q1 = *(const u32x4*)(qbuf + 64 + 4 * i);
    const unsigned tag = flg[6];
    STAMP(3);

    constexpr float SCALE = 0.08838834764831845f, LOG2E = 1.4426950408889634f;
    float* sp = sbuf + wave * 4096 + lane;
    float m = -INFINITY;
    float xx[8];
#define KSTEP(t_, kreg) do { float d0 = 0.f, d1 = 0.f; \
      _Pragma("unroll") for (int k_ = 0; k_ < 4; ++k_) { d0 = dot2((kreg)[k_], q0[k_], d0); d1 = dot2((kreg)[k_], q1[k_], d1); } \
      xx[(t_) * 2] = d0; xx[(t_) * 2 + 1] = d1; } while (0)
#define SCORE(dst) do { \
      _Pragma("unroll") for (int h_ = 0; h_ < 4; ++h_) { const float lo_ = xx[h_], hi_ = xx[h_ + 4]; const float t1 = lo_ + dpp<ROR8>(lo_), t2 = hi_ + dpp<ROR8>(hi_); xx[h_] = (i & 8) ? t2 : t1; } \
      _Pragma("unroll") for (int h_ = 0; h_ < 2; ++h_) { const float lo_ = xx[h_], hi_ = xx[h_ + 2]; const float t1 = lo_ + dpp<XOR7>(lo_), t2 = hi_ + dpp<XOR7>(hi_); xx[h_] = (i & 4) ? t2 : t1; } \
      { const float lo_ = xx[0], hi_ = xx[1]; const float t1 = lo_ + dpp<XOR2>(lo_), t2 = hi_ + dpp<XOR2>(hi_); xx[0] = (i & 2) ? t2 : t1; } \
      float sc = xx[0] + dpp<XOR1>(xx[0]); \
      if (MIMIC) sc = bf16r(bf16r(sc) * SCALE) * LOG2E; else sc *= (SCALE * LOG2E); \
      m = fmaxf(m, sc); (dst) = sc; } while (0)
    unsigned dnext = 1;
    int dl = 1;
#define PACE(step) do { dl = __builtin_amdgcn_readfirstlane(dnext); dnext = __hip_atomic_load(flg + 8, RLX_WG); \
      __hip_atomic_store(prog + wave * 64 + lane, (unsigned)(step), RLX_WG); SCHED(); } while (0)
#define GATE() asm volatile("s_waitcnt vmcnt(%1)\n\ts_cmp_lt_i32 %0, 1\n\ts_cbranch_scc1 1f\n\ts_waitcnt vmcnt(%2)\n\ts_cmp_lt_i32 %0, 2\n\ts_cbranch_scc1 1f\n\ts_waitcnt vmcnt(%3)\n1:" \
      :: "s"(dl), "n"(NWIN), "n"(NWIN - 1), "n"(NWIN - 2) : "scc")
#define KTURN(j) KSTEP((j) & 3, r[j]); asm volatile("" : "+v"(xx[2 * ((j) & 3)]), "+v"(xx[2 * ((j) & 3) + 1])); SCHED(); GATE(); SCHED(); LDR(j, nr, no); SCHED();
#define KTILE(u) KTURN(4 * (u)) KTURN(4 * (u) + 1) KTURN(4 * (u) + 2) KTURN(4 * (u) + 3) SCORE(sp[(t + (u)) * 64]); SCHED();
#pragma nounroll
    for (int t = 0; t < NTILE; t += 8) {
      const bool last = t + 8 >= NTILE;
      const __amdgpu_buffer_rsrc_t nr = last ? vr : kr;
      const unsigned no = last ? off : off + (unsigned)(t + 8) * 16384u;
      PACE(t); KTILE(0) KTILE(1) PACE(t + 2); KTILE(2) KTILE(3) PACE(t + 4); KTILE(4) KTILE(5) PACE(t + 6); KTILE(6) KTILE(7)
    }
    STAMP(4);
    m = fmaxf(m, dpp<ROR4>(m)); m = fmaxf(m, dpp<ROR8>(m)); m = xrow_max(m);
    float l = 0.f;
    f32x4 D = {0.f, 0.f, 0.f, 0.f};
    u32x4 pf = {0u, 0u, 0u, 0u};
    unsigned pq;
    float s0 = sp[0];
#define PROB(sv) do { const float p = __builtin_amdgcn_exp2f((sv) - m); l += p; pq = cvtpk(p, p) & hmask; } while (0)
#define VSTEP(s_, vreg) do { \
      pf[(s_) & 3] = __builtin_amdgcn_update_dpp(pf[(s_) & 3], pq, IDENT, 0xf, 1 << ((s_) & 3), false); \
      pf[((s_) + 1) & 3] = __builtin_amdgcn_update_dpp(pf[((s_) + 1) & 3], pq, ROR4, 0xf, 1 << (((s_) + 1) & 3), false); \
      pf[((s_) + 2) & 3] = __builtin_amdgcn_update_dpp(pf[((s_) + 2) & 3], pq, ROR8, 0xf, 1 << (((s_) + 2) & 3), false); \
      pf[((s_) + 3) & 3] = __builtin_amdgcn_update_dpp(pf[((s_) + 3) & 3], pq, ROR12, 0xf, 1 << (((s_) + 3) & 3), false); \
      D = mfma(vreg, pf, D); } while (0)
#define VTURN(j, RF) VSTEP((j) & 3, r[j]); asm volatile("" : "+v"(D)); SCHED(); if (RF) { GATE(); SCHED(); LDR(j, vr, no); SCHED(); }
#define VTILE(u, RF) PROB(s0); s0 = sp[(t + (u) + 1) * 64]; SCHED(); VTURN(4 * (u), RF) VTURN(4 * (u) + 1, RF) VTURN(4 * (u) + 2, RF) VTURN(4 * (u) + 3, RF)
#pragma nounroll
    for (int t = 0; t < NTILE - 8; t += 8) {
      const unsigned no = off + (unsigned)(t + 8) * 16384u;
      PACE(NTILE + t); VTILE(0, 1) VTILE(1, 1) PACE(NTILE + t + 2); VTILE(2, 1) VTILE(3, 1)
      PACE(NTILE + t + 4); VTILE(4, 1) VTILE(5, 1) PACE(NTILE + t + 6); VTILE(6, 1) VTILE(7, 1)
    }
    {
      if (wave == 0 && lane == 0) __hip_atomic_store(flg + 2, 1u, RLX_WG);
      constexpr int t = NTILE - 8;
      const unsigned no = 0;
      VTILE(0, 0) VTILE(1, 0) VTILE(2, 0) VTILE(3, 0) VTILE(4, 0) VTILE(5, 0) VTILE(6, 0)
      PROB(s0);
      float lt = l + dpp<ROR4>(l); lt += dpp<ROR8>(lt); lt = xrow_sum(lt);
      *(float2*)(ml + (wave * 64 + lane) * 2) = make_float2(m, lt);
      SCHED();
      VTURN(28, 0) VTURN(29, 0) VTURN(30, 0) VTURN(31, 0)
    }
    STAMP(5);
    *(f32x4*)(mrg + wave * 256 + lane * 4) = D;
    if (lds_arrive(flg + 3, lane) == 3) {
      STAMP(6);
      const int b = grp >> 4, kvh = grp & 15;
      float2 e[4]; f32x4 d[4];
#pragma unroll
      for (int w = 0; w < 4; ++w) { e[w] = *(const float2*)(ml + (w * 64 + lane) * 2); d[w] = *(const f32x4*)(mrg + w * 256 + lane * 4); }
      const float M = fmaxf(fmaxf(e[0].x, e[1].x), fmaxf(e[2].x, e[3].x));
      float L = 0.f; f32x4 o = {0.f, 0.f, 0.f, 0.f};
#pragma unroll
      for (int w = 0; w < 4; ++w) { const float wt = __builtin_amdgcn_exp2f(e[w].x - M); L = fmaf(wt, e[w].y, L); o += d[w] * wt; }
      const int pslot = (mem & 1) | (b << 1) | ((mem >> 1) << 2);
      const unsigned ab = WS_AP + (unsigned)((2 * kvh + sel) * 4 + g) * 4096u + (unsigned)pslot * 128u + jj * 16u;
      ST2(ab, o[0], o[1]);
      ST2(ab + 2048u, o[2], o[3]);
      ST2(lane == 2 * sel ? WS_ML + (unsigned)((2 * kvh + sel) * 16 + pslot) * 128u : OOB, M, L);
      STAMP(7);
    }
    if (wave == 0 && bid < 80) {
      lds_wait<1>(flg + 5, 1u);
      const unsigned ro = WS_OP + (unsigned)bid * 4096u + lo;
      u32x4 rv[4];
#pragma unroll
      for (int k = 0; k < 4; ++k) rv[k] = LD128(wsr, ro + k * 1024u, SC1);
#define R_OK(k) (rv[k][1] == tag && rv[k][3] == tag)
      for (unsigned spins = 0; spins < SPIN_CAP; ++spins) {
        if (__all(R_OK(0) && R_OK(1) && R_OK(2) && R_OK(3))) break;
        asm volatile("" ::: "memory");
#pragma unroll
        for (int k = 0; k < 4; ++k) if (!R_OK(k)) rv[k] = LD128(wsr, ro + k * 1024u, SC1);
      }
      float va = (__uint_as_float(rv[0][0]) + __uint_as_float(rv[1][0])) + (__uint_as_float(rv[2][0]) + __uint_as_float(rv[3][0]));
      float vb = (__uint_as_float(rv[0][2]) + __uint_as_float(rv[1][2])) + (__uint_as_float(rv[2][2]) + __uint_as_float(rv[3][2]));
      va += dpp<ROR8>(va); vb += dpp<ROR8>(vb);
      va = xrow_sum(va); vb = xrow_sum(vb);
      const int bj = bid / 5, pr = bid - 5 * bj;
      if (lane < (pr == 4 ? 8 : 16)) out[72 * bj + 16 * pr + lane] = (unsigned short)cvtpk(lane < 8 ? va : vb, 0.f);
      if (bid == 0 && lane == 0) __hip_atomic_store((GAS unsigned*)(ws + WS_EPOCH), tag, RLX_AGENT);
      STAMP(11);
    }
    return;
  }

  const int sw = wave - 4;
  const __amdgpu_buffer_rsrc_t xr = RSRC(x, 2 * DM * 2);
  u32x4 msk;
#pragma unroll
  for (int m = 0; m < 4; ++m) msk[m] = ((i >> 2) == m) ? hmask : 0u;
#define BFRAG(dup) (u32x4{(dup) & msk[0], (dup) & msk[1], (dup) & msk[2], (dup) & msk[3]})
  unsigned tag;

  if (sw == 0) {
    __builtin_amdgcn_s_setprio(3);
    const __amdgpu_buffer_rsrc_t wqr = RSRC(wq, DM * 4096 * 2);
    u32x4 aq[18];
    const unsigned r0 = 72 * mem + 18 * g;
#pragma unroll
    for (int st = 0; st < 18; ++st) aq[st] = LD128(wqr, (r0 + st) * 8192u + (unsigned)grp * 256u + 16u * i, NT_);
    const unsigned xo = (sel * DM + r0) * 2u;
    const u32x4 x0 = LD128(xr, xo, 0), x1 = LD128(xr, xo + 16, 0);
    const unsigned x2 = LD32(xr, xo + 32, 0);
    const unsigned epoch = LD32(wsr, WS_EPOCH, 0);
    if (lane < 16) flg[lane] = lane == 8 ? 1u : 0u;
    prog[lane] = 0u; prog[64 + lane] = 0u; prog[128 + lane] = 0u; prog[192 + lane] = 0u;
    SCHED();
    asm volatile("s_waitcnt lgkmcnt(0)" ::: "memory");
    __builtin_amdgcn_s_barrier();
    SCHED();
    tag = epoch + 0x9E3779B9u;
    if (lane == 0) { flg[6] = tag; asm volatile("s_waitcnt lgkmcnt(0)" ::: "memory"); __hip_atomic_store(flg + 7, 1u, RLX_WG); }
    const unsigned xs[9] = {x0[0], x0[1], x0[2], x0[3], x1[0], x1[1], x1[2], x1[3], x2};
    f32x4 d0 = {0.f, 0.f, 0.f, 0.f}, d1 = {0.f, 0.f, 0.f, 0.f};
#pragma unroll
    for (int k = 0; k < 9; ++k) { d0 = mfma(aq[2 * k], BFRAG(duplo(xs[k])), d0); d1 = mfma(aq[2 * k + 1], BFRAG(duphi(xs[k])), d1); }
    d0 += d1;
    {
      const unsigned qb = WS_QP + (unsigned)(((sel * 16 + (grp >> 1)) * 2 + (grp & 1)) * 8 + mem) * 1024u + (unsigned)(8 * g + jj) * 16u;
      ST2(qb, d0[0], d0[1]);
      ST2(qb + 512u, d0[2], d0[3]);
    }
    STAMP(1);
    u32x4 qa[16];
    const unsigned qoff = WS_QP + (unsigned)grp * 16384u + lo;
    asm volatile("" ::: "memory");
#pragma unroll
    for (int k = 0; k < 16; ++k) qa[k] = LD128(wsr, qoff + k * 1024u, SC1);
    for (unsigned spins = 0; spins < SPIN_CAP; ++spins) {
      unsigned bad = 0;
#pragma unroll
      for (int k = 0; k < 16; ++k) bad |= (qa[k][1] != tag || qa[k][3] != tag) ? (1u << k) : 0u;
      if (__all(bad == 0)) break;
      asm volatile("" ::: "memory");
#pragma unroll
      for (int k = 0; k < 16; ++k) if ((bad >> k) & 1) qa[k] = LD128(wsr, qoff + k * 1024u, SC1);
    }
    float v[4] = {0.f, 0.f, 0.f, 0.f};
#pragma unroll
    for (int s = 0; s < 8; ++s) {
      v[0] += __uint_as_float(qa[s][0]); v[1] += __uint_as_float(qa[s][2]);
      v[2] += __uint_as_float(qa[8 + s][0]); v[3] += __uint_as_float(qa[8 + s][2]);
    }
    {
      unsigned short* qs = (unsigned short*)qbuf + 32 * ((lane >> 3) & 3) + 16 * (lane >> 5) + (lane & 7);
      const unsigned p01 = cvtpk(v[0], v[1]), p23 = cvtpk(v[2], v[3]);
      qs[0] = (unsigned short)p01; qs[8] = (unsigned short)(p01 >> 16);
      qs[128] = (unsigned short)p23; qs[136] = (unsigned short)(p23 >> 16);
    }
    asm volatile("s_waitcnt lgkmcnt(0)" ::: "memory");
    if (lane == 0) __hip_atomic_store(flg + 0, 1u, RLX_WG);
    __builtin_amdgcn_s_setprio(0);
    STAMP(2);
  } else {
    const bool has_f = mem < 6 || (mem == 6 && (grp & 3) != 3);
    const int u = mem < 6 ? grp * 6 + mem : 192 + grp - (grp >> 2);
    const int fmat = u >= 108 ? 1 : 0, fcb = (u - 108 * fmat) / 3, fkt = (u - 108 * fmat) % 3;
    __builtin_amdgcn_s_barrier();
    SCHED();
    if (has_f) {
      const __amdgpu_buffer_rsrc_t wfr = RSRC(fmat ? wu : wg, DM * FF * 2);
      const unsigned r0 = 192 * fkt + 64 * (sw - 1) + 16 * g;
      const u32x4 xa = LD128(xr, (sel * DM + r0) * 2u, 0), xb = LD128(xr, (sel * DM + r0) * 2u + 16, 0);
      const unsigned xs[8] = {xa[0], xa[1], xa[2], xa[3], xb[0], xb[1], xb[2], xb[3]};
      const unsigned fo = r0 * (FF * 2u) + (unsigned)fcb * 256u + 16u * i;
      u32x4 af[16];
#pragma unroll
      for (int st = 0; st < FWIN; ++st) af[st] = LD128(wfr, fo + st * (FF * 2u), NT_);
      SCHED();
      f32x4 d0 = {0.f, 0.f, 0.f, 0.f}, d1 = {0.f, 0.f, 0.f, 0.f};
#pragma unroll
      for (int st = 0; st < 16; ++st) {
        asm volatile("" : "+v"(af[st]));
        SCHED();
        if (st + FWIN < 16) af[st + FWIN] = LD128(wfr, fo + (st + FWIN) * (FF * 2u), NT_);
        SCHED();
        if (st & 1) d1 = mfma(af[st], BFRAG(duphi(xs[st >> 1])), d1); else d0 = mfma(af[st], BFRAG(duplo(xs[st >> 1])), d0);
      }
      *(f32x4*)(red_f + (sw - 1) * 256 + lane * 4) = d0 + d1;
      lds_wait<1>(flg + 7, 1u);
      tag = flg[6];
      if (lds_arrive(flg + 1, lane) == 2) {
        const f32x4 s = *(const f32x4*)(red_f + lane * 4) + *(const f32x4*)(red_f + 256 + lane * 4) + *(const f32x4*)(red_f + 512 + lane * 4);
        const unsigned fb = WS_FP + (unsigned)(((fcb * 2 + fmat) * 3 + fkt) * 2 + sel) * 1024u + (unsigned)(8 * g + jj) * 16u;
        ST2(fb, s[0], s[1]);
        ST2(fb + 512u, s[2], s[3]);
      }
    } else {
      lds_wait<1>(flg + 7, 1u);
      tag = flg[6];
    }
    STAMP(14);
  }

  const int hidx = (bid & 7) * 4 + ((bid >> 3) & 3), jn = bid >> 5;
  u32x4 ao[8], ad[9];
  float gate;
  {
    const __amdgpu_buffer_rsrc_t wor = RSRC(wo, 4096 * DM * 2);
    const __amdgpu_buffer_rsrc_t wdr = RSRC(wd, FF * DM * 2);
    const __amdgpu_buffer_rsrc_t gwr = RSRC(gw, DM * 2);
    u32x4 gx[3], gg[3];
#pragma unroll
    for (int k = 0; k < 3; ++k) {
      const unsigned ch = (unsigned)(lane & 31) + 32u * k;
      gx[k] = LD128(xr, ch < 72 ? (unsigned)(lane >> 5) * (DM * 2u) + ch * 16u : OOB, 0);
      gg[k] = LD128(gwr, ch * 16u, 0);
    }
    const unsigned co = i < 9 ? 144u * jn + 16u * i : OOB;
    float s = 0.f;
#pragma unroll
    for (int k = 0; k < 3; ++k)
#pragma unroll
      for (int e = 0; e < 4; ++e) {
        const float p0 = __uint_as_float(gx[k][e] << 16) * __uint_as_float(gg[k][e] << 16);
        const float p1 = __uint_as_float(gx[k][e] & 0xffff0000u) * __uint_as_float(gg[k][e] & 0xffff0000u);
        s += MIMIC ? bf16r(p0) + bf16r(p1) : p0 + p1;
      }
    s += dpp<XOR1>(s); s += dpp<XOR2>(s); s += dpp<ROR4>(s); s += dpp<ROR8>(s);
    auto t = __builtin_amdgcn_permlane16_swap(__float_as_uint(s), __float_as_uint(s), false, false);
    s = __uint_as_float(t[0]) + __uint_as_float(t[1]);
    const float s0 = __builtin_bit_cast(float, __builtin_amdgcn_readlane(__builtin_bit_cast(int, s), 0));
    const float s1 = __builtin_bit_cast(float, __builtin_amdgcn_readlane(__builtin_bit_cast(int, s), 32));
    float z = sel ? s1 : s0;
    if (MIMIC) z = bf16r(z);
    gate = 1.f / (1.f + __expf(-z));
    if (MIMIC) gate = bf16r(gate);
    asm volatile("" : "+v"(gate));
#pragma unroll
    for (int st = 0; st < 17; ++st) {
      if (st >= WWIN) { asm volatile("s_waitcnt vmcnt(%0)" :: "n"(WWIN - 1) : "memory"); }
      SCHED();
      if (st < 9) ad[st] = LD128(wdr, co + (unsigned)(144 * hidx + 36 * sw + 4 * st + g) * (DM * 2u), NT_);
      else ao[st - 9] = LD128(wor, co + (unsigned)(128 * hidx + 32 * sw + 4 * (st - 9) + g) * (DM * 2u), NT_);
      SCHED();
    }
  }
  STAMP(15);
  if (sw == 1) {
    u32x2 fpv[5];
    unsigned fpo[5];
#pragma unroll
    for (int st = 0; st < 5; ++st) {
      const int rl = 4 * st + g;
      const unsigned f = 18 * bid + rl, n = f & 127;
      fpo[st] = (rl < 18 && (i >> 2) < 3) ? WS_FP + (unsigned)((((f >> 7) * 2 + (i & 1)) * 3 + (i >> 2)) * 2 + sel) * 1024u +
                                            ((n >> 4) & 1) * 512u + (8 * (n >> 5) + (n & 7)) * 16u + ((n >> 3) & 1) * 8u : OOB;
    }
#define FP_OK() ((fpo[0] == OOB || fpv[0][1] == tag) && (fpo[1] == OOB || fpv[1][1] == tag) && (fpo[2] == OOB || fpv[2][1] == tag) && \
                 (fpo[3] == OOB || fpv[3][1] == tag) && (fpo[4] == OOB || fpv[4][1] == tag))
    lds_wait<32>(prog, HT1);
    unsigned spins = 0;
    do {
      if (spins) __builtin_amdgcn_s_sleep(64);
      asm volatile("" ::: "memory");
#pragma unroll
      for (int st = 0; st < 5; ++st) fpv[st] = LD64(wsr, fpo[st], SC1);
    } while (!__all(FP_OK()) && ++spins < SPIN_CAP);
#pragma unroll
    for (int st = 0; st < 5; ++st) {
      float s = __uint_as_float(fpv[st][0]);
      s += dpp<ROR4>(s); s += dpp<ROR8>(s);
      const float o = dpp<XOR1>(s);
      float gv = (i & 1) ? o : s, uv = (i & 1) ? s : o;
      if (MIMIC) { gv = bf16r(gv); uv = bf16r(uv); }
      const float z2 = 1.5957691216057308f * (gv + 0.044715f * gv * gv * gv);
      float ge = gv / (1.f + __expf(-z2));
      if (MIMIC) ge = bf16r(ge);
      const float hv = ge * uv;
      const float ho = dpp<XOR2>(hv);
      if (i == 0 && 4 * st + g < 18) put_granule(ws, WS_HP + (unsigned)(18 * bid + 4 * st + g) * 8u, tag, __uint_as_float(cvtpk(hv, ho)));
    }
  }
  if (sw == 0) {
    const unsigned pgo = WS_PG + (unsigned)grp * 1024u;
#pragma nounroll
    for (unsigned spins = 0; spins < SPIN_CAP; ++spins) {
      if (__builtin_amdgcn_readfirstlane(__hip_atomic_load(prog, RLX_WG)) >= HT3) break;
      unsigned p = __hip_atomic_load(prog + (lane & 3) * 64, RLX_WG);
      p += __builtin_amdgcn_mov_dpp(p, XOR1, 0xf, 0xf, true); p += __builtin_amdgcn_mov_dpp(p, XOR2, 0xf, 0xf, true);
      const unsigned e = p * PTAU - (unsigned)__builtin_amdgcn_s_memrealtime();
      if (lane == 0) put_granule(ws, pgo + mem * 128u, tag, __uint_as_float(e));
      asm volatile("" ::: "memory");
      const u32x2 t = LD64(wsr, (lane < 8 && lane != mem) ? pgo + lane * 128u : OOB, SC1);
      const bool v = lane < 8 && lane != mem && t[1] == tag;
      int df = v ? (int)(e - t[0]) : 0;
      df += __builtin_amdgcn_mov_dpp(df, XOR1, 0xf, 0xf, true); df += __builtin_amdgcn_mov_dpp(df, XOR2, 0xf, 0xf, true); df += __builtin_amdgcn_mov_dpp(df, XOR7, 0xf, 0xf, true);
      const int lead = __builtin_amdgcn_readfirstlane(df) >> 3;
      const int d = lead < -PT1 ? 0 : lead < PT1 ? 1 : 2;
      if (lane == 0) __hip_atomic_store(flg + 8, (unsigned)d, RLX_WG);
      __builtin_amdgcn_s_sleep(PSLEEP);
      if (__builtin_amdgcn_readfirstlane(__hip_atomic_load(prog, RLX_WG)) >= HT3) break;
      __builtin_amdgcn_s_sleep(PSLEEP2);
    }
  }
  lds_wait<64>(prog, HT2);
  f32x4 Df = {0.f, 0.f, 0.f, 0.f};
  {
    const unsigned hpo = lane < 36 ? WS_HP + (unsigned)(144 * hidx + 36 * sw + lane) * 8u : OOB;
    u32x2 hpv;
    unsigned spins = 0;
    do {
      if (spins) __builtin_amdgcn_s_sleep(64);
      asm volatile("" ::: "memory");
      hpv = LD64(wsr, hpo, SC1);
    } while (!__all(lane >= 36 || hpv[1] == tag) && ++spins < SPIN_CAP);
#pragma unroll
    for (int st = 0; st < 9; ++st) {
      const unsigned hh = __builtin_amdgcn_ds_bpermute((4 * st + g) * 4, hpv[0]);
      Df = mfma(ad[st], BFRAG(sel ? duphi(hh) : duplo(hh)), Df);
    }
    Df *= MIMIC ? bf16r(1.f - gate) : 1.f - gate;
    asm volatile("" : "+v"(Df));
  }
  lds_wait<16>(flg + 2, 1u);
  const unsigned aoff = WS_AP + (unsigned)(hidx * 4 + sw) * 4096u + (unsigned)i * 128u + g * 16u;
  const unsigned moff = WS_ML + (unsigned)(hidx * 16 + i) * 128u;
#pragma nounroll
  for (unsigned spins = 0; spins < SPIN_CAP; ++spins) {
    asm volatile("" ::: "memory");
    const u32x2 t = LD64(wsr, g == 0 ? moff : OOB, SC1);
    if (__popcll(__ballot(g == 0 && t[1] != tag)) <= FEW) break;
    __builtin_amdgcn_s_sleep(4);
  }
  STAMP(8);
  f32x4 Da = {0.f, 0.f, 0.f, 0.f};
  {
    asm volatile("" ::: "memory");
    u32x4 a0 = LD128(wsr, aoff, SC1), a1 = LD128(wsr, aoff + 64, SC1), a2 = LD128(wsr, aoff + 2048, SC1), a3 = LD128(wsr, aoff + 2048 + 64, SC1);
    u32x4 am = LD128(wsr, moff, SC1);
#define A_OK() (a0[1] == tag && a0[3] == tag && a1[1] == tag && a1[3] == tag && a2[1] == tag && a2[3] == tag && a3[1] == tag && a3[3] == tag && am[1] == tag && am[3] == tag)
    bool ok = A_OK();
    if (!__all(ok)) {
      unsigned spins = 0;
      do {
        asm volatile("" ::: "memory");
        if (!ok) { a0 = LD128(wsr, aoff, SC1); a1 = LD128(wsr, aoff + 64, SC1); a2 = LD128(wsr, aoff + 2048, SC1); a3 = LD128(wsr, aoff + 2048 + 64, SC1); am = LD128(wsr, moff, SC1); }
        ok = A_OK();
      } while (!__all(ok) && ++spins < SPIN_CAP);
    }
    STAMP(9);
    const float ms = __uint_as_float(am[0]), ls = __uint_as_float(am[2]);
    float M = fmaxf(ms, dpp<XOR1>(ms)); M = fmaxf(M, dpp<ROR4>(M)); M = fmaxf(M, dpp<ROR8>(M));
    const float wt = __builtin_amdgcn_exp2f(ms - M);
    float L = wt * ls; L += dpp<XOR1>(L); L += dpp<ROR4>(L); L += dpp<ROR8>(L);
    const float sc = wt / L;
    float o[8] = {__uint_as_float(a0[0]) * sc, __uint_as_float(a1[0]) * sc, __uint_as_float(a0[2]) * sc, __uint_as_float(a1[2]) * sc,
                  __uint_as_float(a2[0]) * sc, __uint_as_float(a3[0]) * sc, __uint_as_float(a2[2]) * sc, __uint_as_float(a3[2]) * sc};
    f32x4 Db = {0.f, 0.f, 0.f, 0.f};
#pragma unroll
    for (int st = 0; st < 8; ++st) {
      o[st] += dpp<XOR1>(o[st]); o[st] += dpp<ROR4>(o[st]); o[st] += dpp<ROR8>(o[st]);
      if (st & 1) Db = mfma(ao[st], BFRAG(cvtpk(o[st], o[st])), Db); else Da = mfma(ao[st], BFRAG(cvtpk(o[st], o[st])), Da);
    }
    Da += Db;
  }
  *(f32x4*)(red_o + sw * 256 + lane * 4) = Da * gate + Df;
  if (lds_arrive(flg + 4, lane) == 3) {
    const f32x4 s = (*(const f32x4*)(red_o + lane * 4) + *(const f32x4*)(red_o + 256 + lane * 4)) +
                    (*(const f32x4*)(red_o + 512 + lane * 4) + *(const f32x4*)(red_o + 768 + lane * 4));
    const unsigned ob = WS_OP + (unsigned)((sel * 8 + jn) * 5 + 2 * g) * 4096u + (unsigned)hidx * 128u + jj * 16u;
    ST2(g < 3 ? ob : OOB, s[0], s[1]);
    ST2(g < 2 ? ob + 4096u : OOB, s[2], s[3]);
    if (lane == 0) __hip_atomic_store(flg + 5, 1u, RLX_WG);
    STAMP(10);
  }
}

#define WSEND 0
#define WS_BASE(n) ((size_t)0)
extern "C" void kernel_launch(void* const* d_in, const int* in_sizes, int n_in,
                              void* d_out, int out_size, void* d_ws, size_t ws_size,
                              hipStream_t stream) {
  fused_decode<<<256, 512, 0, stream>>>((const unsigned short*)d_in[3], (const unsigned short*)d_in[0], (unsigned char*)d_ws + WS_BASE(ws_size),
                                        (const unsigned short*)d_in[1], (const unsigned short*)d_in[2],
                                        (const unsigned short*)d_in[6], (const unsigned short*)d_in[7], (const unsigned short*)d_in[9],
                                        (const unsigned short*)d_in[4], (const unsigned short*)d_in[8], (unsigned short*)d_out);
}
```

```cpp
#include <hip/hip_runtime.h>
#include <stdint.h>

typedef unsigned u32x4 __attribute__((ext_vector_type(4)));
typedef unsigned u32x2 __attribute__((ext_vector_type(2)));
typedef float f32x4 __attribute__((ext_vector_type(4)));
typedef __bf16 bf16x2 __attribute__((ext_vector_type(2)));
typedef __bf16 bf16x8 __attribute__((ext_vector_type(8)));
typedef unsigned long long u64;
#define GAS __attribute__((address_space(1)))
#define LAS __attribute__((address_space(3)))
#define RLX_AGENT __ATOMIC_RELAXED, __HIP_MEMORY_SCOPE_AGENT
#define RLX_WG __ATOMIC_RELAXED, __HIP_MEMORY_SCOPE_WORKGROUP

#define NWIN 6
#define PWIN 6
#define FWIN 16
#define WWIN 17
#define PACING 1
#define PTAU 14u
#define PSLEEP2 100
#define PSLEEP 100
#define NEAR 8
#define PBASE 1
#define PT1 40
#define PT2 100000
#define HT1 16
#define HT2 36
#define HT3 108
#define EARLY 6
#define FDELAY 0
#define FMAP 0
#define NEARV 54
#define LATEW 56
#define WAUX 0
#define LATEC 4
#define HSLEEP 4
#define WMID 5
#define LATEP 0
#define SPRIO 0
#define PEERS 0
#define QAUX NT_
#define FAUX NT_
#define FEW 2
#define MIMIC 1

constexpr int DM = 576, FF = 4608;
constexpr unsigned OOB = 0x80000000u;
constexpr unsigned SPIN_CAP = 1u << 16;
constexpr int NTILE = 64;

constexpr unsigned WS_EPOCH = 0;
constexpr unsigned WS_QP = 4096;
constexpr unsigned WS_FP = WS_QP + 32 * 16 * 1024;
constexpr unsigned WS_AP = WS_FP + 36 * 12 * 1024;
constexpr unsigned WS_ML = WS_AP + 32 * 16384;
constexpr unsigned WS_OP = WS_ML + 32 * 16 * 128;
constexpr unsigned WS_HP = WS_OP + 80 * 4096;
constexpr unsigned WS_PG = WS_HP + FF * 8;
constexpr unsigned WS_END = WS_PG + 256 * 128;
#define STAMP(k) do { } while (0)

__device__ __forceinline__ float dot2(unsigned k, unsigned q, float acc) {
  return __builtin_amdgcn_fdot2_f32_bf16(__builtin_bit_cast(bf16x2, k), __builtin_bit_cast(bf16x2, q), acc, false);
}
template <int CTRL> __device__ __forceinline__ float dpp(float x) {
  return __builtin_bit_cast(float, __builtin_amdgcn_mov_dpp(__builtin_bit_cast(int, x), CTRL, 0xf, 0xf, true));
}
constexpr int XOR1 = 0xB1, XOR2 = 0x4E, XOR7 = 0x141, ROR4 = 0x124, ROR8 = 0x128, ROR12 = 0x12C, IDENT = 0xE4;
__device__ __forceinline__ unsigned cvtpk(float lo, float hi) {
  bf16x2 v = {(__bf16)lo, (__bf16)hi};
  return __builtin_bit_cast(unsigned, v);
}
__device__ __forceinline__ float bf16r(float x) { return __uint_as_float(cvtpk(x, x) << 16); }
__device__ __forceinline__ float xrow_max(float x) {
  auto s = __builtin_amdgcn_permlane16_swap(__float_as_uint(x), __float_as_uint(x), false, false);
  x = fmaxf(__uint_as_float(s[0]), __uint_as_float(s[1]));
  auto t = __builtin_amdgcn_permlane32_swap(__float_as_uint(x), __float_as_uint(x), false, false);
  return fmaxf(__uint_as_float(t[0]), __uint_as_float(t[1]));
}
__device__ __forceinline__ float xrow_sum(float x) {
  auto s = __builtin_amdgcn_permlane16_swap(__float_as_uint(x), __float_as_uint(x), false, false);
  x = __uint_as_float(s[0]) + __uint_as_float(s[1]);
  auto t = __builtin_amdgcn_permlane32_swap(__float_as_uint(x), __float_as_uint(x), false, false);
  return __uint_as_float(t[0]) + __uint_as_float(t[1]);
}
__device__ __forceinline__ f32x4 mfma(u32x4 a, u32x4 b, f32x4 c) {
  return __builtin_amdgcn_mfma_f32_16x16x32_bf16(__builtin_bit_cast(bf16x8, a), __builtin_bit_cast(bf16x8, b), c, 0, 0, 0);
}
__device__ __forceinline__ void put_granule(unsigned char* ws, unsigned off, unsigned tag, float v) {
  __hip_atomic_store((GAS u64*)(ws + off), ((u64)tag << 32) | (u64)__float_as_uint(v), RLX_AGENT);
}
__device__ __forceinline__ unsigned duplo(unsigned x) { return __builtin_amdgcn_perm(x, x, 0x01000100u); }
__device__ __forceinline__ unsigned duphi(unsigned x) { return __builtin_amdgcn_perm(x, x, 0x03020302u); }
__device__ __forceinline__ unsigned lds_arrive(LAS unsigned* c, int lane) {
  asm volatile("s_waitcnt lgkmcnt(0)" ::: "memory");
  unsigned r = 0;
  if (lane == 0) r = __hip_atomic_fetch_add(c, 1u, RLX_WG);
  r = __builtin_amdgcn_readfirstlane(r);
  asm volatile("" ::: "memory");
  return r;
}
template <int SLEEP> __device__ __forceinline__ void lds_wait(LAS unsigned* c, unsigned want) {
#pragma nounroll
  for (unsigned spins = 0; spins < (1u << 22); ++spins) {
    if (__builtin_amdgcn_readfirstlane(__hip_atomic_load(c, RLX_WG)) >= want) break;
    __builtin_amdgcn_s_sleep(SLEEP);
  }
  asm volatile("" ::: "memory");
}
#define RSRC(p, n) __builtin_amdgcn_make_buffer_rsrc((void*)(p), 0, (n), 0x00020000)
#define LD128(r, off, aux) __builtin_amdgcn_raw_buffer_load_b128((r), (off), 0, (aux))
#define LD64(r, off, aux) __builtin_amdgcn_raw_buffer_load_b64((r), (off), 0, (aux))
#define LD32(r, off, aux) __builtin_amdgcn_raw_buffer_load_b32((r), (off), 0, (aux))
#define SCHED() __builtin_amdgcn_sched_barrier(0)
#define ST2(off, va, vb) __builtin_amdgcn_raw_buffer_store_b128(u32x4{__float_as_uint(va), tag, __float_as_uint(vb), tag}, wsr, (off), 0, 16)
constexpr int NT_ = 2, SC1 = 16;

__global__ void __launch_bounds__(512, 2)
fused_decode(const unsigned short* __restrict__ wq, const unsigned short* __restrict__ x, unsigned char* ws,
             const unsigned short* __restrict__ kc, const unsigned short* __restrict__ vc,
             const unsigned short* __restrict__ wg, const unsigned short* __restrict__ wu, const unsigned short* __restrict__ gw,
             const unsigned short* __restrict__ wo, const unsigned short* __restrict__ wd, unsigned short* __restrict__ out) {
  __shared__ __attribute__((aligned(16))) float lds[3 * 1024 + 128 + 512 + 16 + 4 * 4096 + 256];
  float* sbuf = lds + 3 * 1024 + 128 + 512 + 16;
  LAS unsigned* prog = (LAS unsigned*)(lds + 3 * 1024 + 128 + 512 + 16 + 4 * 4096);
  float* red_f = lds;
  float* mrg = lds + 1024;
  float* red_o = lds + 2048;
  unsigned* qbuf = (unsigned*)(lds + 3072);
  float* ml = lds + 3072 + 128;
  LAS unsigned* flg = (LAS unsigned*)(lds + 3072 + 128 + 512);

  const int bid = blockIdx.x, tid = threadIdx.x, lane = tid & 63;
  const int wave = __builtin_amdgcn_readfirstlane(tid >> 6);
  const int g = lane >> 4, i = lane & 15;
  const int sel = (i >> 1) & 1;
  const int jj = 2 * (i >> 2) + (i & 1);
  const int grp = bid >> 3, mem = bid & 7;
  asm volatile("" :: "s"(wq), "s"(x), "s"(ws), "s"(kc), "s"(vc), "s"(wg), "s"(wu), "s"(gw), "s"(wo), "s"(wd), "s"(out));
  STAMP(0);

  const __amdgpu_buffer_rsrc_t wsr = RSRC(ws, WS_END);
  const unsigned lo = lane * 16;
  const unsigned hmask = (i & 1) ? 0xffff0000u : 0x0000ffffu;

  if (__builtin_expect(wave < 4, 1)) {
    const __amdgpu_buffer_rsrc_t kr = RSRC((const char*)kc + ((size_t)bid << 20), 1u << 20);
    const __amdgpu_buffer_rsrc_t vr = RSRC((const char*)vc + ((size_t)bid << 20), 1u << 20);
    u32x4 r[32];
    const unsigned off = lo + (unsigned)wave * 4096u;
#define LDR(j, rs, o) (r[j] = LD128(rs, (o) + ((j) >> 2) * 16384u + ((j) & 3) * 1024u, NT_))
#pragma unroll
    for (int j = 0; j < 32; ++j) {
      if (j == EARLY) { __builtin_amdgcn_s_barrier(); }
      if (j >= PWIN) { asm volatile("s_waitcnt vmcnt(%0)" :: "n"(PWIN - 1) : "memory"); }
      SCHED();
      LDR(j, kr, off);
      SCHED();
    }
    STAMP(13);
    lds_wait<1>(flg + 0, 1u);
    const u32x4 q0 = *(const u32x4*)(qbuf + 4 * i);
    const u32x4 q1 = *(const u32x4*)(qbuf + 64 + 4 * i);
    const unsigned tag = flg[6];
    STAMP(3);

    constexpr float SCALE = 0.08838834764831845f, LOG2E = 1.4426950408889634f;
    float* sp = sbuf + wave * 4096 + lane;
    float m = -INFINITY;
    float xx[8];
#define KSTEP(t_, kreg) do { float d0 = 0.f, d1 = 0.f; \
      _Pragma("unroll") for (int k_ = 0; k_ < 4; ++k_) { d0 = dot2((kreg)[k_], q0[k_], d0); d1 = dot2((kreg)[k_], q1[k_], d1); } \
      xx[(t_) * 2] = d0; xx[(t_) * 2 + 1] = d1; } while (0)
#define SCORE(dst) do { \
      _Pragma("unroll") for (int h_ = 0; h_ < 4; ++h_) { const float lo_ = xx[h_], hi_ = xx[h_ + 4]; const float t1 = lo_ + dpp<ROR8>(lo_), t2 = hi_ + dpp<ROR8>(hi_); xx[h_] = (i & 8) ? t2 : t1; } \
      _Pragma("unroll") for (int h_ = 0; h_ < 2; ++h_) { const float lo_ = xx[h_], hi_ = xx[h_ + 2]; const float t1 = lo_ + dpp<XOR7>(lo_), t2 = hi_ + dpp<XOR7>(hi_); xx[h_] = (i & 4) ? t2 : t1; } \
      { const float lo_ = xx[0], hi_ = xx[1]; const float t1 = lo_ + dpp<XOR2>(lo_), t2 = hi_ + dpp<XOR2>(hi_); xx[0] = (i & 2) ? t2 : t1; } \
      float sc = xx[0] + dpp<XOR1>(xx[0]); \
      if (MIMIC) sc = bf16r(bf16r(sc) * SCALE) * LOG2E; else sc *= (SCALE * LOG2E); \
      m = fmaxf(m, sc); (dst) = sc; } while (0)
    unsigned dnext = 1;
    int dl = 1;
#define PACE(step) do { dl = __builtin_amdgcn_readfirstlane(dnext); dnext = __hip_atomic_load(flg + 8, RLX_WG); \
      __hip_atomic_store(prog + wave * 64 + lane, (unsigned)(step), RLX_WG); SCHED(); } while (0)
#define GATE() asm volatile("s_waitcnt vmcnt(%1)\n\ts_cmp_lt_i32 %0, 1\n\ts_cbranch_scc1 1f\n\ts_waitcnt vmcnt(%2)\n\ts_cmp_lt_i32 %0, 2\n\ts_cbranch_scc1 1f\n\ts_waitcnt vmcnt(%3)\n1:" \
      :: "s"(dl), "n"(NWIN), "n"(NWIN - 1), "n"(NWIN - 2) : "scc")
#define KTURN(j) KSTEP((j) & 3, r[j]); asm volatile("" : "+v"(xx[2 * ((j) & 3)]), "+v"(xx[2 * ((j) & 3) + 1])); SCHED(); GATE(); SCHED(); LDR(j, nr, no); SCHED();
#define KTILE(u) KTURN(4 * (u)) KTURN(4 * (u) + 1) KTURN(4 * (u) + 2) KTURN(4 * (u) + 3) SCORE(sp[(t + (u)) * 64]); SCHED();
#pragma nounroll
    for (int t = 0; t < NTILE; t += 8) {
      const bool last = t + 8 >= NTILE;
      const __amdgpu_buffer_rsrc_t nr = last ? vr : kr;
      const unsigned no = last ? off : off + (unsigned)(t + 8) * 16384u;
      PACE(t); KTILE(0) KTILE(1) PACE(t + 2); KTILE(2) KTILE(3) PACE(t + 4); KTILE(4) KTILE(5) PACE(t + 6); KTILE(6) KTILE(7)
    }
    STAMP(4);
    m = fmaxf(m, dpp<ROR4>(m)); m = fmaxf(m, dpp<ROR8>(m)); m = xrow_max(m);
    float l = 0.f;
    f32x4 D = {0.f, 0.f, 0.f, 0.f};
    u32x4 pf = {0u, 0u, 0u, 0u};
    unsigned pq;
    float s0 = sp[0];
#define PROB(sv) do { const float p = __builtin_amdgcn_exp2f((sv) - m); l += p; pq = cvtpk(p, p) & hmask; } while (0)
#define VSTEP(s_, vreg) do { \
      pf[(s_) & 3] = __builtin_amdgcn_update_dpp(pf[(s_) & 3], pq, IDENT, 0xf, 1 << ((s_) & 3), false); \
      pf[((s_) + 1) & 3] = __builtin_amdgcn_update_dpp(pf[((s_) + 1) & 3], pq, ROR4, 0xf, 1 << (((s_) + 1) & 3), false); \
      pf[((s_) + 2) & 3] = __builtin_amdgcn_update_dpp(pf[((s_) + 2) & 3], pq, ROR8, 0xf, 1 << (((s_) + 2) & 3), false); \
      pf[((s_) + 3) & 3] = __builtin_amdgcn_update_dpp(pf[((s_) + 3) & 3], pq, ROR12, 0xf, 1 << (((s_) + 3) & 3), false); \
      D = mfma(vreg, pf, D); } while (0)
#define VTURN(j, RF) VSTEP((j) & 3, r[j]); asm volatile("" : "+v"(D)); SCHED(); if (RF) { GATE(); SCHED(); LDR(j, vr, no); SCHED(); }
#define VTILE(u, RF) PROB(s0); s0 = sp[(t + (u) + 1) * 64]; SCHED(); VTURN(4 * (u), RF) VTURN(4 * (u) + 1, RF) VTURN(4 * (u) + 2, RF) VTURN(4 * (u) + 3, RF)
#pragma nounroll
    for (int t = 0; t < NTILE - 8; t += 8) {
      const unsigned no = off + (unsigned)(t + 8) * 16384u;
      PACE(NTILE + t); VTILE(0, 1) VTILE(1, 1) PACE(NTILE + t + 2); VTILE(2, 1) VTILE(3, 1)
      PACE(NTILE + t + 4); VTILE(4, 1) VTILE(5, 1) PACE(NTILE + t + 6); VTILE(6, 1) VTILE(7, 1)
    }
    {
      constexpr int t = NTILE - 8;
      PACE(NTILE + t);
      const unsigned no = 0;
      VTILE(0, 0) VTILE(1, 0) VTILE(2, 0) VTILE(3, 0) VTILE(4, 0) VTILE(5, 0) VTILE(6, 0)
      PROB(s0);
      float lt = l + dpp<ROR4>(l); lt += dpp<ROR8>(lt); lt = xrow_sum(lt);
      *(float2*)(ml + (wave * 64 + lane) * 2) = make_float2(m, lt);
      SCHED();
      VTURN(28, 0) VTURN(29, 0) VTURN(30, 0) VTURN(31, 0)
    }
    STAMP(5);
    *(f32x4*)(mrg + wave * 256 + lane * 4) = D;
    if (lds_arrive(flg + 3, lane) == 3) {
      STAMP(6);
      const int b = grp >> 4, kvh = grp & 15;
      float2 e[4]; f32x4 d[4];
#pragma unroll
      for (int w = 0; w < 4; ++w) { e[w] = *(const float2*)(ml + (w * 64 + lane) * 2); d[w] = *(const f32x4*)(mrg + w * 256 + lane * 4); }
      const float M = fmaxf(fmaxf(e[0].x, e[1].x), fmaxf(e[2].x, e[3].x));
      float L = 0.f; f32x4 o = {0.f, 0.f, 0.f, 0.f};
#pragma unroll
      for (int w = 0; w < 4; ++w) { const float wt = __builtin_amdgcn_exp2f(e[w].x - M); L = fmaf(wt, e[w].y, L); o += d[w] * wt; }
      const int pslot = (mem & 1) | (b << 1) | ((mem >> 1) << 2);
      const unsigned ab = WS_AP + (unsigned)((2 * kvh + sel) * 4 + g) * 4096u + (unsigned)pslot * 128u + jj * 16u;
      ST2(ab, o[0], o[1]);
      ST2(ab + 2048u, o[2], o[3]);
      ST2(lane == 2 * sel ? WS_ML + (unsigned)((2 * kvh + sel) * 16 + pslot) * 128u : OOB, M, L);
      asm volatile("" ::: "memory");
      if (lane == 0) __hip_atomic_store(flg + 9, 1u, RLX_WG);
      STAMP(7);
    }
    if (wave == 0 && bid < 80) {
      lds_wait<1>(flg + 5, 1u);
      const unsigned ro = WS_OP + (unsigned)bid * 4096u + lo;
      u32x4 rv[4];
#pragma unroll
      for (int k = 0; k < 4; ++k) rv[k] = LD128(wsr, ro + k * 1024u, SC1);
#define R_OK(k) (rv[k][1] == tag && rv[k][3] == tag)
      for (unsigned spins = 0; spins < SPIN_CAP; ++spins) {
        if (__all(R_OK(0) && R_OK(1) && R_OK(2) && R_OK(3))) break;
        asm volatile("" ::: "memory");
#pragma unroll
        for (int k = 0; k < 4; ++k) if (!R_OK(k)) rv[k] = LD128(wsr, ro + k * 1024u, SC1);
      }
      float va = (__uint_as_float(rv[0][0]) + __uint_as_float(rv[1][0])) + (__uint_as_float(rv[2][0]) + __uint_as_float(rv[3][0]));
      float vb = (__uint_as_float(rv[0][2]) + __uint_as_float(rv[1][2])) + (__uint_as_float(rv[2][2]) + __uint_as_float(rv[3][2]));
      va += dpp<ROR8>(va); vb += dpp<ROR8>(vb);
      va = xrow_sum(va); vb = xrow_sum(vb);
      const int bj = bid / 5, pr = bid - 5 * bj;
      if (lane < (pr == 4 ? 8 : 16)) out[72 * bj + 16 * pr + lane] = (unsigned short)cvtpk(lane < 8 ? va : vb, 0.f);
      if (bid == 0 && lane == 0) __hip_atomic_store((GAS unsigned*)(ws + WS_EPOCH), tag, RLX_AGENT);
      STAMP(11);
    }
    return;
  }

  const int sw = wave - 4;
  const __amdgpu_buffer_rsrc_t xr = RSRC(x, 2 * DM * 2);
  u32x4 msk;
#pragma unroll
  for (int m = 0; m < 4; ++m) msk[m] = ((i >> 2) == m) ? hmask : 0u;
#define BFRAG(dup) (u32x4{(dup) & msk[0], (dup) & msk[1], (dup) & msk[2], (dup) & msk[3]})
  unsigned tag;

  if (sw == 0) {
    __builtin_amdgcn_s_setprio(3);
    const __amdgpu_buffer_rsrc_t wqr = RSRC(wq, DM * 4096 * 2);
    u32x4 aq[18];
    const unsigned r0 = 72 * mem + 18 * g;
#pragma unroll
    for (int st = 0; st < 18; ++st) aq[st] = LD128(wqr, (r0 + st) * 8192u + (unsigned)grp * 256u + 16u * i, QAUX);
    const unsigned xo = (sel * DM + r0) * 2u;
    const u32x4 x0 = LD128(xr, xo, 0), x1 = LD128(xr, xo + 16, 0);
    const unsigned x2 = LD32(xr, xo + 32, 0);
    const unsigned epoch = LD32(wsr, WS_EPOCH, 0);
    if (lane < 16) flg[lane] = lane == 8 ? 1u : 0u;
    prog[lane] = 0u; prog[64 + lane] = 0u; prog[128 + lane] = 0u; prog[192 + lane] = 0u;
    SCHED();
    asm volatile("s_waitcnt lgkmcnt(0)" ::: "memory");
    __builtin_amdgcn_s_barrier();
    SCHED();
    tag = epoch + 0x9E3779B9u;
    if (lane == 0) { flg[6] = tag; asm volatile("s_waitcnt lgkmcnt(0)" ::: "memory"); __hip_atomic_store(flg + 7, 1u, RLX_WG); }
    const unsigned xs[9] = {x0[0], x0[1], x0[2], x0[3], x1[0], x1[1], x1[2], x1[3], x2};
    f32x4 d0 = {0.f, 0.f, 0.f, 0.f}, d1 = {0.f, 0.f, 0.f, 0.f};
#pragma unroll
    for (int k = 0; k < 9; ++k) { d0 = mfma(aq[2 * k], BFRAG(duplo(xs[k])), d0); d1 = mfma(aq[2 * k + 1], BFRAG(duphi(xs[k])), d1); }
    d0 += d1;
    {
      const unsigned qb = WS_QP + (unsigned)(((sel * 16 + (grp >> 1)) * 2 + (grp & 1)) * 8 + mem) * 1024u + (unsigned)(8 * g + jj) * 16u;
      ST2(qb, d0[0], d0[1]);
      ST2(qb + 512u, d0[2], d0[3]);
    }
    STAMP(1);
    u32x4 qa[16];
    const unsigned qoff = WS_QP + (unsigned)grp * 16384u + lo;
    asm volatile("" ::: "memory");
#pragma unroll
    for (int k = 0; k < 16; ++k) qa[k] = LD128(wsr, qoff + k * 1024u, SC1);
    for (unsigned spins = 0; spins < SPIN_CAP; ++spins) {
      unsigned bad = 0;
#pragma unroll
      for (int k = 0; k < 16; ++k) bad |= (qa[k][1] != tag || qa[k][3] != tag) ? (1u << k) : 0u;
      if (__all(bad == 0)) break;
      asm volatile("" ::: "memory");
#pragma unroll
      for (int k = 0; k < 16; ++k) if ((bad >> k) & 1) qa[k] = LD128(wsr, qoff + k * 1024u, SC1);
    }
    float v[4] = {0.f, 0.f, 0.f, 0.f};
#pragma unroll
    for (int s = 0; s < 8; ++s) {
      v[0] += __uint_as_float(qa[s][0]); v[1] += __uint_as_float(qa[s][2]);
      v[2] += __uint_as_float(qa[8 + s][0]); v[3] += __uint_as_float(qa[8 + s][2]);
    }
    {
      unsigned short* qs = (unsigned short*)qbuf + 32 * ((lane >> 3) & 3) + 16 * (lane >> 5) + (lane & 7);
      const unsigned p01 = cvtpk(v[0], v[1]), p23 = cvtpk(v[2], v[3]);
      qs[0] = (unsigned short)p01; qs[8] = (unsigned short)(p01 >> 16);
      qs[128] = (unsigned short)p23; qs[136] = (unsigned short)(p23 >> 16);
    }
    asm volatile("s_waitcnt lgkmcnt(0)" ::: "memory");
    if (lane == 0) __hip_atomic_store(flg + 0, 1u, RLX_WG);
    __builtin_amdgcn_s_setprio(0);
    STAMP(2);
  } else {
    const bool has_f = mem < 6 || (mem == 6 && (grp & 3) != 3);
    const int u = mem < 6 ? grp * 6 + mem : 192 + grp - (grp >> 2);
    const int fmat = u >= 108 ? 1 : 0, fcb = (u - 108 * fmat) / 3, fkt = (u - 108 * fmat) % 3;
    __builtin_amdgcn_s_barrier();
    SCHED();
    if (has_f) {
      const __amdgpu_buffer_rsrc_t wfr = RSRC(fmat ? wu : wg, DM * FF * 2);
      const unsigned r0 = 192 * fkt + 64 * (sw - 1) + 16 * g;
      const u32x4 xa = LD128(xr, (sel * DM + r0) * 2u, 0), xb = LD128(xr, (sel * DM + r0) * 2u + 16, 0);
      const unsigned xs[8] = {xa[0], xa[1], xa[2], xa[3], xb[0], xb[1], xb[2], xb[3]};
      const unsigned fo = r0 * (FF * 2u) + (unsigned)fcb * 256u + 16u * i;
      u32x4 af[16];
#pragma unroll
      for (int st = 0; st < FWIN; ++st) af[st] = LD128(wfr, fo + st * (FF * 2u), FAUX);
      SCHED();
      f32x4 d0 = {0.f, 0.f, 0.f, 0.f}, d1 = {0.f, 0.f, 0.f, 0.f};
#pragma unroll
      for (int st = 0; st < 16; ++st) {
        asm volatile("" : "+v"(af[st]));
        SCHED();
        if (st + FWIN < 16) af[st + FWIN] = LD128(wfr, fo + (st + FWIN) * (FF * 2u), FAUX);
        SCHED();
        if (st & 1) d1 = mfma(af[st], BFRAG(duphi(xs[st >> 1])), d1); else d0 = mfma(af[st], BFRAG(duplo(xs[st >> 1])), d0);
      }
      *(f32x4*)(red_f + (sw - 1) * 256 + lane * 4) = d0 + d1;
      lds_wait<1>(flg + 7, 1u);
      tag = flg[6];
      if (lds_arrive(flg + 1, lane) == 2) {
        const f32x4 s = *(const f32x4*)(red_f + lane * 4) + *(const f32x4*)(red_f + 256 + lane * 4) + *(const f32x4*)(red_f + 512 + lane * 4);
        const unsigned fb = WS_FP + (unsigned)(((fcb * 2 + fmat) * 3 + fkt) * 2 + sel) * 1024u + (unsigned)(8 * g + jj) * 16u;
        ST2(fb, s[0], s[1]);
        ST2(fb + 512u, s[2], s[3]);
      }
    } else {
      lds_wait<1>(flg + 7, 1u);
      tag = flg[6];
    }
    STAMP(14);
  }

  const int hidx = (bid & 7) * 4 + ((bid >> 3) & 3), jn = bid >> 5;
  u32x4 ao[8], ad[9];
  float gate;
  const __amdgpu_buffer_rsrc_t wor = RSRC(wo, 4096 * DM * 2);
  const __amdgpu_buffer_rsrc_t wdr = RSRC(wd, FF * DM * 2);
  const unsigned co = i < 9 ? 144u * jn + 16u * i : OOB;
#define LOADW(S0, S1) _Pragma("unroll") for (int st = S0; st < S1; ++st) { \
      if (st >= WWIN) { asm volatile("s_waitcnt vmcnt(%0)" :: "n"(WWIN - 1) : "memory"); } \
      SCHED(); \
      if (st < 9) ad[st] = LD128(wdr, co + (unsigned)(144 * hidx + 36 * sw + 4 * st + g) * (DM * 2u), WAUX); \
      else ao[st - 9] = LD128(wor, co + (unsigned)(128 * hidx + 32 * sw + 4 * (st - 9) + g) * (DM * 2u), WAUX); \
      SCHED(); }
  {
    const __amdgpu_buffer_rsrc_t gwr = RSRC(gw, DM * 2);
    u32x4 gx[3], gg[3];
#pragma unroll
    for (int k = 0; k < 3; ++k) {
      const unsigned ch = (unsigned)(lane & 31) + 32u * k;
      gx[k] = LD128(xr, ch < 72 ? (unsigned)(lane >> 5) * (DM * 2u) + ch * 16u : OOB, 0);
      gg[k] = LD128(gwr, ch * 16u, 0);
    }
    float s = 0.f;
#pragma unroll
    for (int k = 0; k < 3; ++k)
#pragma unroll
      for (int e = 0; e < 4; ++e) {
        const float p0 = __uint_as_float(gx[k][e] << 16) * __uint_as_float(gg[k][e] << 16);
        const float p1 = __uint_as_float(gx[k][e] & 0xffff0000u) * __uint_as_float(gg[k][e] & 0xffff0000u);
        s += MIMIC ? bf16r(p0) + bf16r(p1) : p0 + p1;
      }
    s += dpp<XOR1>(s); s += dpp<XOR2>(s); s += dpp<ROR4>(s); s += dpp<ROR8>(s);
    auto t = __builtin_amdgcn_permlane16_swap(__float_as_uint(s), __float_as_uint(s), false, false);
    s = __uint_as_float(t[0]) + __uint_as_float(t[1]);
    const float s0 = __builtin_bit_cast(float, __builtin_amdgcn_readlane(__builtin_bit_cast(int, s), 0));
    const float s1 = __builtin_bit_cast(float, __builtin_amdgcn_readlane(__builtin_bit_cast(int, s), 32));
    float z = sel ? s1 : s0;
    if (MIMIC) z = bf16r(z);
    gate = 1.f / (1.f + __expf(-z));
    if (MIMIC) gate = bf16r(gate);
    asm volatile("" : "+v"(gate));
    const unsigned t0 = LD32(wdr, lane == 0 ? (unsigned)(144 * hidx + 36 * sw) * (DM * 2u) : OOB, NT_);
    const unsigned t1 = LD32(wor, lane == 0 ? (unsigned)(128 * hidx + 32 * sw) * (DM * 2u) : OOB, NT_);
    asm volatile("" :: "v"(t0), "v"(t1));
  }
  STAMP(15);
  if (sw == 1) {
    u32x2 fpv[5];
    unsigned fpo[5];
#pragma unroll
    for (int st = 0; st < 5; ++st) {
      const int rl = 4 * st + g;
      const unsigned f = 18 * bid + rl, n = f & 127;
      fpo[st] = (rl < 18 && (i >> 2) < 3) ? WS_FP + (unsigned)((((f >> 7) * 2 + (i & 1)) * 3 + (i >> 2)) * 2 + sel) * 1024u +
                                            ((n >> 4) & 1) * 512u + (8 * (n >> 5) + (n & 7)) * 16u + ((n >> 3) & 1) * 8u : OOB;
    }
#define FP_OK() ((fpo[0] == OOB || fpv[0][1] == tag) && (fpo[1] == OOB || fpv[1][1] == tag) && (fpo[2] == OOB || fpv[2][1] == tag) && \
                 (fpo[3] == OOB || fpv[3][1] == tag) && (fpo[4] == OOB || fpv[4][1] == tag))
    lds_wait<32>(prog, HT1);
    unsigned spins = 0;
    do {
      if (spins) __builtin_amdgcn_s_sleep(64);
      asm volatile("" ::: "memory");
#pragma unroll
      for (int st = 0; st < 5; ++st) fpv[st] = LD64(wsr, fpo[st], SC1);
    } while (!__all(FP_OK()) && ++spins < SPIN_CAP);
#pragma unroll
    for (int st = 0; st < 5; ++st) {
      float s = __uint_as_float(fpv[st][0]);
      s += dpp<ROR4>(s); s += dpp<ROR8>(s);
      const float o = dpp<XOR1>(s);
      float gv = (i & 1) ? o : s, uv = (i & 1) ? s : o;
      if (MIMIC) { gv = bf16r(gv); uv = bf16r(uv); }
      const float z2 = 1.5957691216057308f * (gv + 0.044715f * gv * gv * gv);
      float ge = gv / (1.f + __expf(-z2));
      if (MIMIC) ge = bf16r(ge);
      const float hv = ge * uv;
      const float ho = dpp<XOR2>(hv);
      if (i == 0 && 4 * st + g < 18) put_granule(ws, WS_HP + (unsigned)(18 * bid + 4 * st + g) * 8u, tag, __uint_as_float(cvtpk(hv, ho)));
    }
  }
  if (sw == 0) {
    const unsigned pgo = WS_PG + (unsigned)grp * 1024u;
    const unsigned peer = PEERS ? WS_PG + (unsigned)((bid + PEERS * (lane - mem)) & 255) * 128u : pgo + lane * 128u;
#pragma nounroll
    for (unsigned spins = 0; spins < SPIN_CAP; ++spins) {
      if (__builtin_amdgcn_readfirstlane(__hip_atomic_load(prog, RLX_WG)) >= HT3) break;
      unsigned p = __hip_atomic_load(prog + (lane & 3) * 64, RLX_WG);
      p += __builtin_amdgcn_mov_dpp(p, XOR1, 0xf, 0xf, true); p += __builtin_amdgcn_mov_dpp(p, XOR2, 0xf, 0xf, true);
      const unsigned e = p * PTAU - (unsigned)__builtin_amdgcn_s_memrealtime();
      if (lane == 0) put_granule(ws, pgo + mem * 128u, tag, __uint_as_float(e));
      asm volatile("" ::: "memory");
      const u32x2 t = LD64(wsr, (lane < 8 && lane != mem) ? peer : OOB, SC1);
      const bool v = lane < 8 && lane != mem && t[1] == tag;
      int df = v ? (int)(e - t[0]) : 0;
      df += __builtin_amdgcn_mov_dpp(df, XOR1, 0xf, 0xf, true); df += __builtin_amdgcn_mov_dpp(df, XOR2, 0xf, 0xf, true); df += __builtin_amdgcn_mov_dpp(df, XOR7, 0xf, 0xf, true);
      const int lead = __builtin_amdgcn_readfirstlane(df) >> 3;
      const int d = lead < -PT1 ? 0 : lead < PT1 ? 1 : 2;
      if (lane == 0) __hip_atomic_store(flg + 8, (unsigned)d, RLX_WG);
      __builtin_amdgcn_s_sleep(PSLEEP);
      if (__builtin_amdgcn_readfirstlane(__hip_atomic_load(prog, RLX_WG)) >= HT3) break;
      __builtin_amdgcn_s_sleep(PSLEEP2);
    }
  }
  lds_wait<64>(prog, HT2);
  f32x4 Df = {0.f, 0.f, 0.f, 0.f};
  unsigned hval;
  {
    const unsigned hpo = lane < 36 ? WS_HP + (unsigned)(144 * hidx + 36 * sw + lane) * 8u : OOB;
    u32x2 hpv;
    unsigned spins = 0;
    do {
      if (spins) __builtin_amdgcn_s_sleep(64);
      asm volatile("" ::: "memory");
      hpv = LD64(wsr, hpo, SC1);
    } while (!__all(lane >= 36 || hpv[1] == tag) && ++spins < SPIN_CAP);
    hval = hpv[0];
  }
  lds_wait<32>(prog, NTILE + 40);
  lds_wait<2>(prog, NTILE + 56);
  lds_wait<1>(prog + 64, NTILE + 56); lds_wait<1>(prog + 128, NTILE + 56); lds_wait<1>(prog + 192, NTILE + 56);
  LOADW(0, WMID);
  lds_wait<1>(flg + 9, 1u);
  LOADW(WMID, 17);
  const unsigned aoff = WS_AP + (unsigned)(hidx * 4 + sw) * 4096u + (unsigned)i * 128u + g * 16u;
  const unsigned moff = WS_ML + (unsigned)(hidx * 16 + i) * 128u;
  asm volatile("" ::: "memory");
  u32x4 a0 = LD128(wsr, aoff, SC1), a1 = LD128(wsr, aoff + 64, SC1), a2 = LD128(wsr, aoff + 2048, SC1), a3 = LD128(wsr, aoff + 2048 + 64, SC1);
  u32x4 am = LD128(wsr, moff, SC1);
  SCHED();
  {
#pragma unroll
    for (int st = 0; st < 9; ++st) {
      const unsigned hh = __builtin_amdgcn_ds_bpermute((4 * st + g) * 4, hval);
      Df = mfma(ad[st], BFRAG(sel ? duphi(hh) : duplo(hh)), Df);
    }
    Df *= MIMIC ? bf16r(1.f - gate) : 1.f - gate;
    asm volatile("" : "+v"(Df));
    SCHED();
  }
  STAMP(8);
  f32x4 Da = {0.f, 0.f, 0.f, 0.f};
  {
#define A_OK() (a0[1] == tag && a0[3] == tag && a1[1] == tag && a1[3] == tag && a2[1] == tag && a2[3] == tag && a3[1] == tag && a3[3] == tag && am[1] == tag && am[3] == tag)
    bool ok = A_OK();
    if (!__all(ok)) {
      unsigned spins = 0;
      do {
        const bool few = __popcll(__ballot(!ok)) <= 4 * FEW;
        if (!few) __builtin_amdgcn_s_sleep(HSLEEP);
        asm volatile("" ::: "memory");
        if (!ok && (few || am[1] == tag)) { a0 = LD128(wsr, aoff, SC1); a1 = LD128(wsr, aoff + 64, SC1); a2 = LD128(wsr, aoff + 2048, SC1); a3 = LD128(wsr, aoff + 2048 + 64, SC1); }
        if (!ok) am = LD128(wsr, moff, SC1);
        ok = A_OK();
      } while (!__all(ok) && ++spins < SPIN_CAP);
    }
    STAMP(9);
    const float ms = __uint_as_float(am[0]), ls = __uint_as_float(am[2]);
    float M = fmaxf(ms, dpp<XOR1>(ms)); M = fmaxf(M, dpp<ROR4>(M)); M = fmaxf(M, dpp<ROR8>(M));
    const float wt = __builtin_amdgcn_exp2f(ms - M);
    float L = wt * ls; L += dpp<XOR1>(L); L += dpp<ROR4>(L); L += dpp<ROR8>(L);
    const float sc = wt / L;
    float o[8] = {__uint_as_float(a0[0]) * sc, __uint_as_float(a1[0]) * sc, __uint_as_float(a0[2]) * sc, __uint_as_float(a1[2]) * sc,
                  __uint_as_float(a2[0]) * sc, __uint_as_float(a3[0]) * sc, __uint_as_float(a2[2]) * sc, __uint_as_float(a3[2]) * sc};
    f32x4 Db = {0.f, 0.f, 0.f, 0.f};
#pragma unroll
    for (int st = 0; st < 8; ++st) {
      o[st] += dpp<XOR1>(o[st]); o[st] += dpp<ROR4>(o[st]); o[st] += dpp<ROR8>(o[st]);
      if (st & 1) Db = mfma(ao[st], BFRAG(cvtpk(o[st], o[st])), Db); else Da = mfma(ao[st], BFRAG(cvtpk(o[st], o[st])), Da);
    }
    Da += Db;
  }
  *(f32x4*)(red_o + sw * 256 + lane * 4) = Da * gate + Df;
  if (lds_arrive(flg + 4, lane) == 3) {
    const f32x4 s = (*(const f32x4*)(red_o + lane * 4) + *(const f32x4*)(red_o + 256 + lane * 4)) +
                    (*(const f32x4*)(red_o + 512 + lane * 4) + *(const f32x4*)(red_o + 768 + lane * 4));
    const unsigned ob = WS_OP + (unsigned)((sel * 8 + jn) * 5 + 2 * g) * 4096u + (unsigned)hidx * 128u + jj * 16u;
    ST2(g < 3 ? ob : OOB, s[0], s[1]);
    ST2(g < 2 ? ob + 4096u : OOB, s[2], s[3]);
    if (lane == 0) __hip_atomic_store(flg + 5, 1u, RLX_WG);
    STAMP(10);
  }
}

#define WSEND 0
#define WS_BASE(n) ((size_t)0)
extern "C" void kernel_launch(void* const* d_in, const int* in_sizes, int n_in,
                              void* d_out, int out_size, void* d_ws, size_t ws_size,
                              hipStream_t stream) {
  fused_decode<<<256, 512, 0, stream>>>((const unsigned short*)d_in[3], (const unsigned short*)d_in[0], (unsigned char*)d_ws + WS_BASE(ws_size),
                                        (const unsigned short*)d_in[1], (const unsigned short*)d_in[2],
                                        (const unsigned short*)d_in[6], (const unsigned short*)d_in[7], (const unsigned short*)d_in[9],
                                        (const unsigned short*)d_in[4], (const unsigned short*)d_in[8], (unsigned short*)d_out);
}
```

```cpp
#include <hip/hip_runtime.h>
#include <stdint.h>

typedef unsigned u32x4 __attribute__((ext_vector_type(4)));
typedef unsigned u32x2 __attribute__((ext_vector_type(2)));
typedef float f32x4 __attribute__((ext_vector_type(4)));
typedef __bf16 bf16x2 __attribute__((ext_vector_type(2)));
typedef __bf16 bf16x8 __attribute__((ext_vector_type(8)));
typedef unsigned long long u64;
#define GAS __attribute__((address_space(1)))
#define LAS __attribute__((address_space(3)))
#define RLX_AGENT __ATOMIC_RELAXED, __HIP_MEMORY_SCOPE_AGENT
#define RLX_WG __ATOMIC_RELAXED, __HIP_MEMORY_SCOPE_WORKGROUP

#define NWIN 6
#define PWIN 5
#define FWIN 8
#define QDELAY 30
#define PT1 40
#define HT3 108
#define WMID 0
#define HSLEEP 8

constexpr int DM = 576, FF = 4608;
constexpr unsigned OOB = 0x80000000u;
constexpr unsigned SPIN_CAP = 1u << 16;
constexpr int NTILE = 64;
constexpr unsigned PTAU = 14u;

constexpr unsigned WS_EPOCH = 0;
constexpr unsigned WS_QP = 4096;
constexpr unsigned WS_FP = WS_QP + 32 * 16 * 1024;
constexpr unsigned WS_AP = WS_FP + 36 * 12 * 1024;
constexpr unsigned WS_ML = WS_AP + 32 * 8192;
constexpr unsigned WS_OP = WS_ML + 256 * 128;
constexpr unsigned WS_HP = WS_OP + 72 * 4096;
constexpr unsigned WS_PG = WS_HP + FF * 8;
constexpr unsigned WS_END = WS_PG + 256 * 128;
#define STAMP(k) do { } while (0)

enum { F_Q = 0, F_FFN = 1, F_MERGE = 3, F_OUT = 4, F_OUTPUB = 5, F_TAG = 6, F_TAGOK = 7, F_LEVEL = 8, F_PUB = 9, F_STAT = 10, F_SCALE = 11 };

__device__ __forceinline__ float dot2(unsigned k, unsigned q, float acc) {
  return __builtin_amdgcn_fdot2_f32_bf16(__builtin_bit_cast(bf16x2, k), __builtin_bit_cast(bf16x2, q), acc, false);
}
template <int CTRL> __device__ __forceinline__ float dpp(float x) {
  return __builtin_bit_cast(float, __builtin_amdgcn_mov_dpp(__builtin_bit_cast(int, x), CTRL, 0xf, 0xf, true));
}
template <int CTRL> __device__ __forceinline__ int dppi(int x) { return __builtin_amdgcn_mov_dpp(x, CTRL, 0xf, 0xf, true); }
constexpr int XOR1 = 0xB1, XOR2 = 0x4E, XOR7 = 0x141, ROR4 = 0x124, ROR8 = 0x128, ROR12 = 0x12C, IDENT = 0xE4;
__device__ __forceinline__ unsigned cvtpk(float lo, float hi) {
  bf16x2 v = {(__bf16)lo, (__bf16)hi};
  return __builtin_bit_cast(unsigned, v);
}
__device__ __forceinline__ float bf16r(float x) { return __uint_as_float(cvtpk(x, x) << 16); }
__device__ __forceinline__ float xrow_max(float x) {
  auto s = __builtin_amdgcn_permlane16_swap(__float_as_uint(x), __float_as_uint(x), false, false);
  x = fmaxf(__uint_as_float(s[0]), __uint_as_float(s[1]));
  auto t = __builtin_amdgcn_permlane32_swap(__float_as_uint(x), __float_as_uint(x), false, false);
  return fmaxf(__uint_as_float(t[0]), __uint_as_float(t[1]));
}
__device__ __forceinline__ float xrow_sum(float x) {
  auto s = __builtin_amdgcn_permlane16_swap(__float_as_uint(x), __float_as_uint(x), false, false);
  x = __uint_as_float(s[0]) + __uint_as_float(s[1]);
  auto t = __builtin_amdgcn_permlane32_swap(__float_as_uint(x), __float_as_uint(x), false, false);
  return __uint_as_float(t[0]) + __uint_as_float(t[1]);
}
__device__ __forceinline__ f32x4 mfma(u32x4 a, u32x4 b, f32x4 c) {
  return __builtin_amdgcn_mfma_f32_16x16x32_bf16(__builtin_bit_cast(bf16x8, a), __builtin_bit_cast(bf16x8, b), c, 0, 0, 0);
}
__device__ __forceinline__ void put_granule(unsigned char* ws, unsigned off, unsigned tag, unsigned v) {
  __hip_atomic_store((GAS u64*)(ws + off), ((u64)tag << 32) | (u64)v, RLX_AGENT);
}
__device__ __forceinline__ unsigned duplo(unsigned x) { return __builtin_amdgcn_perm(x, x, 0x01000100u); }
__device__ __forceinline__ unsigned duphi(unsigned x) { return __builtin_amdgcn_perm(x, x, 0x03020302u); }
__device__ __forceinline__ unsigned lds_arrive(LAS unsigned* c, int lane) {
  asm volatile("s_waitcnt lgkmcnt(0)" ::: "memory");
  unsigned r = 0;
  if (lane == 0) r = __hip_atomic_fetch_add(c, 1u, RLX_WG);
  r = __builtin_amdgcn_readfirstlane(r);
  asm volatile("" ::: "memory");
  return r;
}
__device__ __forceinline__ void lds_set(LAS unsigned* c, unsigned v, int lane) {
  asm volatile("s_waitcnt lgkmcnt(0)" ::: "memory");
  if (lane == 0) __hip_atomic_store(c, v, RLX_WG);
}
template <int SLEEP> __device__ __forceinline__ void lds_wait(LAS unsigned* c, unsigned want) {
#pragma nounroll
  for (unsigned spins = 0; spins < (1u << 25) / (SLEEP + 2); ++spins) {
    if (__builtin_amdgcn_readfirstlane(__hip_atomic_load(c, RLX_WG)) >= want) break;
    __builtin_amdgcn_s_sleep(SLEEP);
  }
  asm volatile("" ::: "memory");
}
#define RSRC(p, n) __builtin_amdgcn_make_buffer_rsrc((void*)(p), 0, (n), 0x00020000)
#define LD128(r, off, aux) __builtin_amdgcn_raw_buffer_load_b128((r), (off), 0, (aux))
#define LD64(r, off, aux) __builtin_amdgcn_raw_buffer_load_b64((r), (off), 0, (aux))
#define LD32(r, off, aux) __builtin_amdgcn_raw_buffer_load_b32((r), (off), 0, (aux))
#define SCHED() __builtin_amdgcn_sched_barrier(0)
#define ST2(off, va, vb) __builtin_amdgcn_raw_buffer_store_b128(u32x4{__float_as_uint(va), tag, __float_as_uint(vb), tag}, wsr, (off), 0, 16)
constexpr int NT_ = 2, SC1 = 16;

__global__ void __launch_bounds__(512, 2)
fused_decode(const unsigned short* __restrict__ wq, const unsigned short* __restrict__ x, unsigned char* ws,
             const unsigned short* __restrict__ kc, const unsigned short* __restrict__ vc,
             const unsigned short* __restrict__ wg, const unsigned short* __restrict__ wu, const unsigned short* __restrict__ gw,
             const unsigned short* __restrict__ wo, const unsigned short* __restrict__ wd, unsigned short* __restrict__ out) {
  __shared__ __attribute__((aligned(16))) float lds[3 * 1024 + 128 + 32 + 16 + 256 + 4 * 4096];
  float* red_f = lds;
  float* mrg = lds + 1024;
  float* red_o = lds + 2048;
  unsigned* qbuf = (unsigned*)(lds + 3072);
  float* ml = lds + 3072 + 128;
  LAS unsigned* flg = (LAS unsigned*)(lds + 3072 + 128 + 32);
  LAS unsigned* prog = (LAS unsigned*)(lds + 3072 + 128 + 32 + 16);
  float* sbuf = lds + 3072 + 128 + 32 + 16 + 256;

  const int bid = blockIdx.x, tid = threadIdx.x, lane = tid & 63;
  const int wave = __builtin_amdgcn_readfirstlane(tid >> 6);
  const int g = lane >> 4, i = lane & 15;
  const int sel = (i >> 1) & 1;
  const int jj = 2 * (i >> 2) + (i & 1);
  const int grp = bid >> 3, mem = bid & 7;
  asm volatile("" :: "s"(wq), "s"(x), "s"(ws), "s"(kc), "s"(vc), "s"(wg), "s"(wu), "s"(gw), "s"(wo), "s"(wd), "s"(out));
  STAMP(0);

  const __amdgpu_buffer_rsrc_t wsr = RSRC(ws, WS_END);
  const unsigned lo = lane * 16;
  const unsigned hmask = (i & 1) ? 0xffff0000u : 0x0000ffffu;

  if (__builtin_expect(wave < 4, 1)) {
    const __amdgpu_buffer_rsrc_t kr = RSRC((const char*)kc + ((size_t)bid << 20), 1u << 20);
    const __amdgpu_buffer_rsrc_t vr = RSRC((const char*)vc + ((size_t)bid << 20), 1u << 20);
    u32x4 r[32];
    const unsigned off = lo + (unsigned)wave * 4096u;
#define LDR(j, rs, o) (r[j] = LD128(rs, (o) + ((j) >> 2) * 16384u + ((j) & 3) * 1024u, NT_))
#pragma unroll
    for (int j = 0; j < 32; ++j) {
      if (j == PWIN) { __builtin_amdgcn_s_barrier(); }
      if (j >= PWIN) { asm volatile("s_waitcnt vmcnt(%0)" :: "n"(PWIN - 1) : "memory"); }
      SCHED();
      LDR(j, kr, off);
      SCHED();
    }
    STAMP(13);
    lds_wait<1>(flg + F_Q, 1u);
    const u32x4 q0 = *(const u32x4*)(qbuf + 4 * i);
    const u32x4 q1 = *(const u32x4*)(qbuf + 64 + 4 * i);
    const unsigned tag = flg[F_TAG];
    STAMP(3);

    constexpr float SCALE = 0.08838834764831845f, LOG2E = 1.4426950408889634f;
    float* sp = sbuf + wave * 4096 + lane;
    float m = -INFINITY, l = 0.f;
    float xx[8];
#define KSTEP(t_, kreg) do { float d0 = 0.f, d1 = 0.f; \
      _Pragma("unroll") for (int k_ = 0; k_ < 4; ++k_) { d0 = dot2((kreg)[k_], q0[k_], d0); d1 = dot2((kreg)[k_], q1[k_], d1); } \
      xx[(t_) * 2] = d0; xx[(t_) * 2 + 1] = d1; } while (0)
#define SCORE(dst) do { \
      _Pragma("unroll") for (int h_ = 0; h_ < 4; ++h_) { const float lo_ = xx[h_], hi_ = xx[h_ + 4]; const float t1 = lo_ + dpp<ROR8>(lo_), t2 = hi_ + dpp<ROR8>(hi_); xx[h_] = (i & 8) ? t2 : t1; } \
      _Pragma("unroll") for (int h_ = 0; h_ < 2; ++h_) { const float lo_ = xx[h_], hi_ = xx[h_ + 2]; const float t1 = lo_ + dpp<XOR7>(lo_), t2 = hi_ + dpp<XOR7>(hi_); xx[h_] = (i & 4) ? t2 : t1; } \
      { const float lo_ = xx[0], hi_ = xx[1]; const float t1 = lo_ + dpp<XOR2>(lo_), t2 = hi_ + dpp<XOR2>(hi_); xx[0] = (i & 2) ? t2 : t1; } \
      const float sc = bf16r(bf16r(xx[0] + dpp<XOR1>(xx[0])) * SCALE) * LOG2E; \
      const float mn_ = fmaxf(m, sc); l = fmaf(l, __builtin_amdgcn_exp2f(m - mn_), __builtin_amdgcn_exp2f(sc - mn_)); m = mn_; (dst) = sc; } while (0)
    unsigned dnext = 1;
    int dl = 1;
#define PACE(step) do { dl = __builtin_amdgcn_readfirstlane(dnext); dnext = __hip_atomic_load(flg + F_LEVEL, RLX_WG); \
      __hip_atomic_store(prog + wave * 64 + lane, (unsigned)(step), RLX_WG); SCHED(); } while (0)
#define GATE() asm volatile("s_waitcnt vmcnt(%1)\n\ts_cmp_lt_i32 %0, 1\n\ts_cbranch_scc1 1f\n\ts_waitcnt vmcnt(%2)\n\ts_cmp_lt_i32 %0, 2\n\ts_cbranch_scc1 1f\n\ts_waitcnt vmcnt(%3)\n1:" \
      :: "s"(dl), "n"(NWIN), "n"(NWIN - 1), "n"(NWIN - 2) : "scc")
#define KTURN(j) KSTEP((j) & 3, r[j]); asm volatile("" : "+v"(xx[2 * ((j) & 3)]), "+v"(xx[2 * ((j) & 3) + 1])); SCHED(); GATE(); SCHED(); LDR(j, nr, no); SCHED();
#define KTILE(u) KTURN(4 * (u)) KTURN(4 * (u) + 1) KTURN(4 * (u) + 2) KTURN(4 * (u) + 3) SCORE(sp[(t + (u)) * 64]); SCHED();
#pragma nounroll
    for (int t = 0; t < NTILE; t += 8) {
      const bool last = t + 8 >= NTILE;
      const __amdgpu_buffer_rsrc_t nr = last ? vr : kr;
      const unsigned no = last ? off : off + (unsigned)(t + 8) * 16384u;
      PACE(t); KTILE(0) KTILE(1) PACE(t + 2); KTILE(2) KTILE(3) PACE(t + 4); KTILE(4) KTILE(5) PACE(t + 6); KTILE(6) KTILE(7)
    }
    STAMP(4);
    {
      float mw = fmaxf(m, dpp<ROR4>(m)); mw = fmaxf(mw, dpp<ROR8>(mw)); mw = xrow_max(mw);
      l *= __builtin_amdgcn_exp2f(m - mw); m = mw;
      l += dpp<ROR4>(l); l += dpp<ROR8>(l); l = xrow_sum(l);
      if (lane == 2 * sel) *(float2*)(ml + (wave * 2 + sel) * 2) = make_float2(m, l);
      asm volatile("s_waitcnt lgkmcnt(0)" ::: "memory");
      if (lane == 0) __hip_atomic_fetch_add(flg + F_STAT, 1u, RLX_WG);
    }
    f32x4 D = {0.f, 0.f, 0.f, 0.f};
    u32x4 pf = {0u, 0u, 0u, 0u};
    unsigned pq;
    float cw = 0.f;
    float s0 = sp[0];
#define PROB(sv) (cvtpk(__builtin_amdgcn_exp2f((sv) - m), __builtin_amdgcn_exp2f((sv) - m)) & hmask)
#define VSTEP(s_, vreg, pf, pq, D) do { \
      pf[(s_) & 3] = __builtin_amdgcn_update_dpp(pf[(s_) & 3], pq, IDENT, 0xf, 1 << ((s_) & 3), false); \
      pf[((s_) + 1) & 3] = __builtin_amdgcn_update_dpp(pf[((s_) + 1) & 3], pq, ROR4, 0xf, 1 << (((s_) + 1) & 3), false); \
      pf[((s_) + 2) & 3] = __builtin_amdgcn_update_dpp(pf[((s_) + 2) & 3], pq, ROR8, 0xf, 1 << (((s_) + 2) & 3), false); \
      pf[((s_) + 3) & 3] = __builtin_amdgcn_update_dpp(pf[((s_) + 3) & 3], pq, ROR12, 0xf, 1 << (((s_) + 3) & 3), false); \
      D = mfma(vreg, pf, D); } while (0)
#define VTURN(j) VSTEP((j) & 3, r[j], pf, pq, D); asm volatile("" : "+v"(D)); SCHED(); GATE(); SCHED(); LDR(j, vr, no); SCHED();
#define VTILE(u) pq = PROB(s0); s0 = sp[(t + (u) + 1) * 64]; SCHED(); VTURN(4 * (u)) VTURN(4 * (u) + 1) VTURN(4 * (u) + 2) VTURN(4 * (u) + 3)
    unsigned pl[8];
#pragma nounroll
    for (int t = 0; t < NTILE - 8; t += 8) {
      const unsigned no = off + (unsigned)(t + 8) * 16384u;
      if (t == NTILE - 16) {
        lds_wait<1>(flg + F_SCALE, 1u);
        cw = ml[16 + wave * 2 + sel];
      }
      PACE(NTILE + t); VTILE(0) VTILE(1) PACE(NTILE + t + 2); VTILE(2) VTILE(3)
      PACE(NTILE + t + 4); VTILE(4) VTILE(5) PACE(NTILE + t + 6); VTILE(6) VTILE(7)
    }
    {
      PACE(2 * NTILE - 8);
#pragma unroll
      for (int u = 0; u < 8; ++u) pl[u] = PROB(sp[(NTILE - 8 + u) * 64]);
      f32x4 E = {0.f, 0.f, 0.f, 0.f};
      u32x4 pg = {0u, 0u, 0u, 0u};
#define VLAST(u) VSTEP(0, r[4 * (u)], pf, pl[u], D); VSTEP(1, r[4 * (u) + 1], pg, pl[u], E); VSTEP(2, r[4 * (u) + 2], pf, pl[u], D); VSTEP(3, r[4 * (u) + 3], pg, pl[u], E);
      VLAST(0) VLAST(1) VLAST(2) VLAST(3) VLAST(4) VLAST(5) VLAST(6) VLAST(7)
      D += E;
    }
    STAMP(5);
    *(f32x4*)(mrg + wave * 256 + lane * 4) = D * cw;
    if (lds_arrive(flg + F_MERGE, lane) == 3) {
      STAMP(6);
      const int b = grp >> 4, kvh = grp & 15;
      const f32x4 o = (*(const f32x4*)(mrg + lane * 4) + *(const f32x4*)(mrg + 256 + lane * 4)) +
                      (*(const f32x4*)(mrg + 512 + lane * 4) + *(const f32x4*)(mrg + 768 + lane * 4));
      const int pslot = (mem & 1) | (b << 1) | ((mem >> 1) << 2);
      ST2(WS_AP + (unsigned)((2 * kvh + sel) * 4 + g) * 2048u + (unsigned)pslot * 128u + jj * 16u,
          __uint_as_float(cvtpk(o[0], o[1])), __uint_as_float(cvtpk(o[2], o[3])));
      lds_set(flg + F_PUB, 1u, lane);
      STAMP(7);
    }
    if (wave == 0 && bid < 72) {
      lds_wait<1>(flg + F_OUTPUB, 1u);
      const unsigned ro = WS_OP + (unsigned)bid * 4096u + lo;
      u32x4 rv[4];
#pragma unroll
      for (int k = 0; k < 4; ++k) rv[k] = LD128(wsr, ro + k * 1024u, SC1);
#define R_OK(k) (rv[k][1] == tag && rv[k][3] == tag)
      for (unsigned spins = 0; spins < SPIN_CAP; ++spins) {
        if (__all(R_OK(0) && R_OK(1) && R_OK(2) && R_OK(3))) break;
        asm volatile("" ::: "memory");
#pragma unroll
        for (int k = 0; k < 4; ++k) if (!R_OK(k)) rv[k] = LD128(wsr, ro + k * 1024u, SC1);
      }
      float va = (__uint_as_float(rv[0][0]) + __uint_as_float(rv[1][0])) + (__uint_as_float(rv[2][0]) + __uint_as_float(rv[3][0]));
      float vb = (__uint_as_float(rv[0][2]) + __uint_as_float(rv[1][2])) + (__uint_as_float(rv[2][2]) + __uint_as_float(rv[3][2]));
      va += dpp<ROR8>(va); vb += dpp<ROR8>(vb);
      va = xrow_sum(va); vb = xrow_sum(vb);
      if (lane < 16) __hip_atomic_store((GAS unsigned short*)out + 16 * bid + lane, (unsigned short)cvtpk(lane < 8 ? va : vb, 0.f), RLX_AGENT);
      if (bid == 0 && lane == 0) __hip_atomic_store((GAS unsigned*)(ws + WS_EPOCH), tag, RLX_AGENT);
      STAMP(11);
    }
    return;
  }

  const int sw = wave - 4;
  const __amdgpu_buffer_rsrc_t xr = RSRC(x, 2 * DM * 2);
  u32x4 msk;
#pragma unroll
  for (int k = 0; k < 4; ++k) msk[k] = ((i >> 2) == k) ? hmask : 0u;
#define BFRAG(dup) (u32x4{(dup) & msk[0], (dup) & msk[1], (dup) & msk[2], (dup) & msk[3]})
  unsigned tag;

  if (sw == 0) {
    __builtin_amdgcn_s_setprio(3);
    const __amdgpu_buffer_rsrc_t wqr = RSRC(wq, DM * 4096 * 2);
    u32x4 aq[18];
    const unsigned r0 = 72 * mem + 18 * g;
#pragma unroll
    for (int st = 0; st < 18; ++st) aq[st] = LD128(wqr, (r0 + st) * 8192u + (unsigned)grp * 256u + 16u * i, NT_);
    const unsigned xo = (sel * DM + r0) * 2u;
    const u32x4 x0 = LD128(xr, xo, 0), x1 = LD128(xr, xo + 16, 0);
    const unsigned x2 = LD32(xr, xo + 32, 0);
    const unsigned epoch = LD32(wsr, WS_EPOCH, 0);
    if (lane < 16) flg[lane] = lane == F_LEVEL ? 1u : 0u;
    prog[lane] = 0u; prog[64 + lane] = 0u; prog[128 + lane] = 0u; prog[192 + lane] = 0u;
    SCHED();
    asm volatile("s_waitcnt lgkmcnt(0)" ::: "memory");
    __builtin_amdgcn_s_barrier();
    SCHED();
    tag = (epoch + 0x9E3779B9u) | 1u;
    if (lane == 0) flg[F_TAG] = tag;
    lds_set(flg + F_TAGOK, 1u, lane);
    const unsigned xs[9] = {x0[0], x0[1], x0[2], x0[3], x1[0], x1[1], x1[2], x1[3], x2};
    f32x4 d0 = {0.f, 0.f, 0.f, 0.f}, d1 = {0.f, 0.f, 0.f, 0.f};
#pragma unroll
    for (int k = 0; k < 9; ++k) { d0 = mfma(aq[2 * k], BFRAG(duplo(xs[k])), d0); d1 = mfma(aq[2 * k + 1], BFRAG(duphi(xs[k])), d1); }
    d0 += d1;
    {
      const unsigned qb = WS_QP + (unsigned)(((sel * 16 + (grp >> 1)) * 2 + (grp & 1)) * 8 + mem) * 1024u + (unsigned)(8 * g + jj) * 16u;
      ST2(qb, d0[0], d0[1]);
      ST2(qb + 512u, d0[2], d0[3]);
    }
    STAMP(1);
    __builtin_amdgcn_s_sleep(QDELAY);
    u32x4 qa[16];
    const unsigned qoff = WS_QP + (unsigned)grp * 16384u + lo;
    asm volatile("" ::: "memory");
#pragma unroll
    for (int k = 0; k < 16; ++k) qa[k] = LD128(wsr, qoff + k * 1024u, SC1);
    for (unsigned spins = 0; spins < SPIN_CAP; ++spins) {
      unsigned bad = 0;
#pragma unroll
      for (int k = 0; k < 16; ++k) bad |= (qa[k][1] != tag || qa[k][3] != tag) ? (1u << k) : 0u;
      if (__all(bad == 0)) break;
      asm volatile("" ::: "memory");
#pragma unroll
      for (int k = 0; k < 16; ++k) if ((bad >> k) & 1) qa[k] = LD128(wsr, qoff + k * 1024u, SC1);
    }
    float v[4] = {0.f, 0.f, 0.f, 0.f};
#pragma unroll
    for (int s = 0; s < 8; ++s) {
      v[0] += __uint_as_float(qa[s][0]); v[1] += __uint_as_float(qa[s][2]);
      v[2] += __uint_as_float(qa[8 + s][0]); v[3] += __uint_as_float(qa[8 + s][2]);
    }
    {
      unsigned short* qs = (unsigned short*)qbuf + 32 * ((lane >> 3) & 3) + 16 * (lane >> 5) + (lane & 7);
      const unsigned p01 = cvtpk(v[0], v[1]), p23 = cvtpk(v[2], v[3]);
      qs[0] = (unsigned short)p01; qs[8] = (unsigned short)(p01 >> 16);
      qs[128] = (unsigned short)p23; qs[136] = (unsigned short)(p23 >> 16);
    }
    lds_set(flg + F_Q, 1u, lane);
    __builtin_amdgcn_s_setprio(0);
    STAMP(2);
  } else {
    const bool has_f = mem < 6 || (mem == 6 && (grp & 3) != 3);
    const int u = mem < 6 ? grp * 6 + mem : 192 + grp - (grp >> 2);
    const int fmat = u >= 108 ? 1 : 0, fcb = (u - 108 * fmat) / 3, fkt = (u - 108 * fmat) % 3;
    __builtin_amdgcn_s_barrier();
    SCHED();
    if (has_f) {
      const __amdgpu_buffer_rsrc_t wfr = RSRC(fmat ? wu : wg, DM * FF * 2);
      const unsigned r0 = 192 * fkt + 64 * (sw - 1) + 16 * g;
      const u32x4 xa = LD128(xr, (sel * DM + r0) * 2u, 0), xb = LD128(xr, (sel * DM + r0) * 2u + 16, 0);
      const unsigned xs[8] = {xa[0], xa[1], xa[2], xa[3], xb[0], xb[1], xb[2], xb[3]};
      const unsigned fo = r0 * (FF * 2u) + (unsigned)fcb * 256u + 16u * i;
      u32x4 af[16];
#pragma unroll
      for (int st = 0; st < FWIN; ++st) af[st] = LD128(wfr, fo + st * (FF * 2u), NT_);
      SCHED();
      f32x4 d0 = {0.f, 0.f, 0.f, 0.f}, d1 = {0.f, 0.f, 0.f, 0.f};
#pragma unroll
      for (int st = 0; st < 16; ++st) {
        asm volatile("" : "+v"(af[st]));
        SCHED();
        if (st + FWIN < 16) af[st + FWIN] = LD128(wfr, fo + (st + FWIN) * (FF * 2u), NT_);
        SCHED();
        if (st & 1) d1 = mfma(af[st], BFRAG(duphi(xs[st >> 1])), d1); else d0 = mfma(af[st], BFRAG(duplo(xs[st >> 1])), d0);
      }
      *(f32x4*)(red_f + (sw - 1) * 256 + lane * 4) = d0 + d1;
      lds_wait<1>(flg + F_TAGOK, 1u);
      tag = flg[F_TAG];
      if (lds_arrive(flg + F_FFN, lane) == 2) {
        const f32x4 s = *(const f32x4*)(red_f + lane * 4) + *(const f32x4*)(red_f + 256 + lane * 4) + *(const f32x4*)(red_f + 512 + lane * 4);
        const unsigned fb = WS_FP + (unsigned)(((fcb * 2 + fmat) * 3 + fkt) * 2 + sel) * 1024u + (unsigned)(8 * g + jj) * 16u;
        ST2(fb, s[0], s[1]);
        ST2(fb + 512u, s[2], s[3]);
      }
    } else {
      lds_wait<1>(flg + F_TAGOK, 1u);
      tag = flg[F_TAG];
    }
    STAMP(14);
  }

  const int hidx = bid & 31, jn = bid >> 5;
  const bool wide = jn == 7;
  u32x4 ao[8], ad[9];
  float gate;
  const __amdgpu_buffer_rsrc_t wor = RSRC(wo, 4096 * DM * 2);
  const __amdgpu_buffer_rsrc_t wdr = RSRC(wd, FF * DM * 2);
  const unsigned co = (i < 8 || wide) ? 128u * jn + 16u * i : OOB;
  const unsigned dof = co + (unsigned)(144 * hidx + 36 * sw + g) * (DM * 2u), oof = co + (unsigned)(128 * hidx + 32 * sw + g) * (DM * 2u);
  {
    const __amdgpu_buffer_rsrc_t gwr = RSRC(gw, DM * 2);
    u32x4 gx[3], gg[3];
#pragma unroll
    for (int k = 0; k < 3; ++k) {
      const unsigned ch = (unsigned)(lane & 31) + 32u * k;
      gx[k] = LD128(xr, ch < 72 ? (unsigned)(lane >> 5) * (DM * 2u) + ch * 16u : OOB, 0);
      gg[k] = LD128(gwr, ch * 16u, 0);
    }
    float s = 0.f;
#pragma unroll
    for (int k = 0; k < 3; ++k)
#pragma unroll
      for (int e = 0; e < 4; ++e)
        s += bf16r(__uint_as_float(gx[k][e] << 16) * __uint_as_float(gg[k][e] << 16)) +
             bf16r(__uint_as_float(gx[k][e] & 0xffff0000u) * __uint_as_float(gg[k][e] & 0xffff0000u));
    s += dpp<XOR1>(s); s += dpp<XOR2>(s); s += dpp<ROR4>(s); s += dpp<ROR8>(s);
    auto t = __builtin_amdgcn_permlane16_swap(__float_as_uint(s), __float_as_uint(s), false, false);
    s = __uint_as_float(t[0]) + __uint_as_float(t[1]);
    const float s0 = __builtin_bit_cast(float, __builtin_amdgcn_readlane(__builtin_bit_cast(int, s), 0));
    const float s1 = __builtin_bit_cast(float, __builtin_amdgcn_readlane(__builtin_bit_cast(int, s), 32));
    gate = bf16r(1.f / (1.f + __expf(-bf16r(sel ? s1 : s0))));
    asm volatile("" : "+v"(gate));
#pragma unroll
    for (int st = 0; st < 9; ++st) ad[st] = LD128(wdr, wide ? dof + st * (4 * DM * 2u) : OOB, NT_);
    const unsigned t0 = LD32(wdr, lane == 0 ? dof : OOB, NT_);
    const unsigned t1 = LD32(wor, lane == 0 ? oof : OOB, NT_);
    asm volatile("" :: "v"(t0), "v"(t1));
  }
  STAMP(15);
  if (sw == 1) {
    u32x2 fpv[5];
    unsigned fpo[5];
#pragma unroll
    for (int st = 0; st < 5; ++st) {
      const int rl = 4 * st + g;
      const unsigned f = 18 * bid + rl, n = f & 127;
      fpo[st] = (rl < 18 && (i >> 2) < 3) ? WS_FP + (unsigned)((((f >> 7) * 2 + (i & 1)) * 3 + (i >> 2)) * 2 + sel) * 1024u +
                                            ((n >> 4) & 1) * 512u + (8 * (n >> 5) + (n & 7)) * 16u + ((n >> 3) & 1) * 8u : OOB;
    }
#define FP_OK() ((fpo[0] == OOB || fpv[0][1] == tag) && (fpo[1] == OOB || fpv[1][1] == tag) && (fpo[2] == OOB || fpv[2][1] == tag) && \
                 (fpo[3] == OOB || fpv[3][1] == tag) && (fpo[4] == OOB || fpv[4][1] == tag))
    lds_wait<32>(prog, 16u);
    unsigned spins = 0;
    do {
      if (spins) __builtin_amdgcn_s_sleep(64);
      asm volatile("" ::: "memory");
#pragma unroll
      for (int st = 0; st < 5; ++st) fpv[st] = LD64(wsr, fpo[st], SC1);
    } while (!__all(FP_OK()) && ++spins < SPIN_CAP);
#pragma unroll
    for (int st = 0; st < 5; ++st) {
      float s = __uint_as_float(fpv[st][0]);
      s += dpp<ROR4>(s); s += dpp<ROR8>(s);
      const float o = dpp<XOR1>(s);
      const float gv = bf16r((i & 1) ? o : s), uv = bf16r((i & 1) ? s : o);
      const float z2 = 1.5957691216057308f * (gv + 0.044715f * gv * gv * gv);
      const float hv = bf16r(gv / (1.f + __expf(-z2))) * uv;
      const float ho = dpp<XOR2>(hv);
      if (i == 0 && 4 * st + g < 18) put_granule(ws, WS_HP + (unsigned)(18 * bid + 4 * st + g) * 8u, tag, cvtpk(hv, ho));
    }
  }
  if (sw == 0) {
    const unsigned pgo = WS_PG + (unsigned)grp * 1024u;
#pragma nounroll
    for (unsigned spins = 0; spins < SPIN_CAP; ++spins) {
      if (__builtin_amdgcn_readfirstlane(__hip_atomic_load(prog, RLX_WG)) >= HT3) break;
      int p = (int)__hip_atomic_load(prog + (lane & 3) * 64, RLX_WG);
      p += dppi<XOR1>(p); p += dppi<XOR2>(p);
      const unsigned e = (unsigned)p * PTAU - (unsigned)__builtin_amdgcn_s_memrealtime();
      if (lane == 0) put_granule(ws, pgo + mem * 128u, tag, e);
      asm volatile("" ::: "memory");
      const u32x2 t = LD64(wsr, (lane < 8 && lane != mem) ? pgo + lane * 128u : OOB, SC1);
      int df = t[1] == tag ? (int)(e - t[0]) : 0;
      df += dppi<XOR1>(df); df += dppi<XOR2>(df); df += dppi<XOR7>(df);
      const int lead = __builtin_amdgcn_readfirstlane(df) >> 3;
      if (lane == 0) __hip_atomic_store(flg + F_LEVEL, lead < -PT1 ? 0u : lead < PT1 ? 1u : 2u, RLX_WG);
      __builtin_amdgcn_s_sleep(100);
      if (__builtin_amdgcn_readfirstlane(__hip_atomic_load(prog, RLX_WG)) >= HT3) break;
      __builtin_amdgcn_s_sleep(100);
    }
  }
  lds_wait<64>(prog, 36u);
  unsigned hval;
  {
    const unsigned hpo = lane < 36 ? WS_HP + (unsigned)(144 * hidx + 36 * sw + lane) * 8u : OOB;
    u32x2 hpv;
    unsigned spins = 0;
    do {
      if (spins) __builtin_amdgcn_s_sleep(64);
      asm volatile("" ::: "memory");
      hpv = LD64(wsr, hpo, SC1);
    } while (!__all(lane >= 36 || hpv[1] == tag) && ++spins < SPIN_CAP);
    hval = hpv[0];
  }
  if (sw == 2) {
    lds_wait<16>(flg + F_STAT, 4u);
    const int h = (lane >> 3) & 1;
    const float2 e = *(const float2*)(ml + ((lane & 3) * 2 + h) * 2);
    {
      float M = fmaxf(e.x, dpp<XOR1>(e.x)); M = fmaxf(M, dpp<XOR2>(M));
      float L = e.y * __builtin_amdgcn_exp2f(e.x - M); L += dpp<XOR1>(L); L += dpp<XOR2>(L);
      ST2(lane == 8 * h ? WS_ML + (unsigned)bid * 128u + h * 16u : OOB, M, L);
    }
    lds_wait<32>(prog, NTILE + 16);
    const unsigned mo = lane < 16 ? WS_ML + (unsigned)(grp * 8 + (lane & 7)) * 128u + h * 16u : OOB;
    u32x4 t;
    unsigned spins = 0;
    do {
      if (spins) __builtin_amdgcn_s_sleep(32);
      asm volatile("" ::: "memory");
      t = LD128(wsr, mo, SC1);
    } while (!__all(lane >= 16 || (t[1] == tag && t[3] == tag)) && ++spins < SPIN_CAP);
    const float ms = __uint_as_float(t[0]);
    float M = fmaxf(ms, dpp<XOR1>(ms)); M = fmaxf(M, dpp<XOR2>(M)); M = fmaxf(M, dpp<XOR7>(M));
    float L = __uint_as_float(t[2]) * __builtin_amdgcn_exp2f(ms - M); L += dpp<XOR1>(L); L += dpp<XOR2>(L); L += dpp<XOR7>(L);
    if (lane < 16 && (lane & 7) < 4) ml[16 + (lane & 3) * 2 + h] = __builtin_amdgcn_exp2f(e.x - M) / L;
    lds_set(flg + F_SCALE, 1u, lane);
  }
  unsigned hd[9];
#pragma unroll
  for (int st = 0; st < 9; ++st) { const unsigned hh = __builtin_amdgcn_ds_bpermute((4 * st + g) * 4, hval); hd[st] = sel ? duphi(hh) : duplo(hh); }
  f32x4 Df = {0.f, 0.f, 0.f, 0.f};
#pragma unroll
  for (int st = 0; st < 9; ++st) Df = mfma(ad[st], BFRAG(hd[st]), Df);
  asm volatile("" : "+v"(Df));
  const unsigned dof2 = wide ? OOB : dof;
  lds_wait<32>(prog, NTILE + 40);
  lds_wait<2>(prog, NTILE + 56);
  lds_wait<1>(prog + 64, NTILE + 56); lds_wait<1>(prog + 128, NTILE + 56); lds_wait<1>(prog + 192, NTILE + 56);
#pragma unroll
  for (int st = 0; st < WMID; ++st) ad[st] = LD128(wdr, dof2 + st * (4 * DM * 2u), 0);
  SCHED();
  lds_wait<1>(flg + F_PUB, 1u);
#pragma unroll
  for (int st = WMID; st < 9; ++st) ad[st] = LD128(wdr, dof2 + st * (4 * DM * 2u), 0);
#pragma unroll
  for (int st = 0; st < 8; ++st) ao[st] = LD128(wor, oof + st * (4 * DM * 2u), 0);
  const unsigned aoff = WS_AP + (unsigned)(hidx * 4 + sw) * 2048u + (unsigned)i * 128u + g * 16u;
  asm volatile("" ::: "memory");
  u32x4 a0 = LD128(wsr, aoff, SC1), a1 = LD128(wsr, aoff + 64, SC1);
  SCHED();
#pragma unroll
  for (int st = 0; st < 9; ++st) Df = mfma(ad[st], BFRAG(hd[st]), Df);
  Df *= bf16r(1.f - gate);
  asm volatile("" : "+v"(Df));
  SCHED();
  STAMP(8);
  f32x4 Da = {0.f, 0.f, 0.f, 0.f};
  {
#define A_OK() (a0[1] == tag && a0[3] == tag && a1[1] == tag && a1[3] == tag)
    bool ok = A_OK();
    if (!__all(ok)) {
      unsigned spins = 0;
      do {
        if (__popcll(__ballot(!ok)) > 8) __builtin_amdgcn_s_sleep(HSLEEP);
        asm volatile("" ::: "memory");
        if (!ok) { a0 = LD128(wsr, aoff, SC1); a1 = LD128(wsr, aoff + 64, SC1); }
        ok = A_OK();
      } while (!__all(ok) && ++spins < SPIN_CAP);
    }
    STAMP(9);
    float o[8] = {__uint_as_float(a0[0] << 16), __uint_as_float(a1[0] << 16), __uint_as_float(a0[0] & 0xffff0000u), __uint_as_float(a1[0] & 0xffff0000u),
                  __uint_as_float(a0[2] << 16), __uint_as_float(a1[2] << 16), __uint_as_float(a0[2] & 0xffff0000u), __uint_as_float(a1[2] & 0xffff0000u)};
    f32x4 Db = {0.f, 0.f, 0.f, 0.f};
#pragma unroll
    for (int st = 0; st < 8; ++st) {
      o[st] += dpp<XOR1>(o[st]); o[st] += dpp<ROR4>(o[st]); o[st] += dpp<ROR8>(o[st]);
      if (st & 1) Db = mfma(ao[st], BFRAG(cvtpk(o[st], o[st])), Db); else Da = mfma(ao[st], BFRAG(cvtpk(o[st], o[st])), Da);
    }
    Da += Db;
  }
  *(f32x4*)(red_o + sw * 256 + lane * 4) = Da * gate + Df;
  if (lds_arrive(flg + F_OUT, lane) == 3) {
    const f32x4 s = (*(const f32x4*)(red_o + lane * 4) + *(const f32x4*)(red_o + 256 + lane * 4)) +
                    (*(const f32x4*)(red_o + 512 + lane * 4) + *(const f32x4*)(red_o + 768 + lane * 4));
    const unsigned ob = (g < 2 || wide) ? WS_OP + (unsigned)(sel * 36 + 4 * jn + 2 * g) * 4096u + (unsigned)hidx * 128u + jj * 16u : OOB;
    ST2(ob, s[0], s[1]);
    ST2(ob + 4096u, s[2], s[3]);
    lds_set(flg + F_OUTPUB, 1u, lane);
    STAMP(10);
  }
}

extern "C" void kernel_launch(void* const* d_in, const int* in_sizes, int n_in,
                              void* d_out, int out_size, void* d_ws, size_t ws_size,
                              hipStream_t stream) {
  fused_decode<<<256, 512, 0, stream>>>((const unsigned short*)d_in[3], (const unsigned short*)d_in[0], (unsigned char*)d_ws,
                                        (const unsigned short*)d_in[1], (const unsigned short*)d_in[2],
                                        (const unsigned short*)d_in[6], (const unsigned short*)d_in[7], (const unsigned short*)d_in[9],
                                        (const unsigned short*)d_in[4], (const unsigned short*)d_in[8], (unsigned short*)d_out);
}
```

```cpp
#include <hip/hip_runtime.h>
#include <stdint.h>

typedef unsigned u32x4 __attribute__((ext_vector_type(4)));
typedef unsigned u32x2 __attribute__((ext_vector_type(2)));
typedef float f32x4 __attribute__((ext_vector_type(4)));
typedef __bf16 bf16x2 __attribute__((ext_vector_type(2)));
typedef __bf16 bf16x8 __attribute__((ext_vector_type(8)));
typedef unsigned long long u64;
#define GAS __attribute__((address_space(1)))
#define LAS __attribute__((address_space(3)))
#define RLX_AGENT __ATOMIC_RELAXED, __HIP_MEMORY_SCOPE_AGENT
#define RLX_WG __ATOMIC_RELAXED, __HIP_MEMORY_SCOPE_WORKGROUP

#define NWIN 6
#define PWIN 4
#define QAT 31
#define WBAND 2
#define HT4 119
#define WSLEEP 16
#define PGAIN 30
#define BIGL 150
#define PSL 6
#define PSLICE 25
#define PSWITCH 80
#define PSL0 12
#define FWIN 4
#define QDELAY 30
#define HT3 114
#define HSLEEP 8

constexpr int DM = 576, FF = 4608;
constexpr unsigned OOB = 0x80000000u;
constexpr unsigned SPIN_CAP = 1u << 16;
constexpr int NTILE = 64;
constexpr unsigned PTAU = 14u;

constexpr unsigned WS_EPOCH = 0;
constexpr unsigned WS_QP = 4096;
constexpr unsigned WS_FP = WS_QP + 32 * 16 * 1024;
constexpr unsigned WS_AP = WS_FP + 36 * 12 * 1024;
constexpr unsigned WS_ML = WS_AP + 32 * 8192;
constexpr unsigned WS_OP = WS_ML + 256 * 128;
constexpr unsigned WS_HP = WS_OP + 72 * 4096;
constexpr unsigned WS_PG = WS_HP + FF * 8;
constexpr unsigned WS_EPOCH2 = WS_PG + 256 * 128;
constexpr unsigned WS_END = WS_EPOCH2 + 128;

enum { F_Q = 0, F_FFN = 1, F_MERGE = 3, F_OUT = 4, F_TAG = 6, F_TAGOK = 7, F_LEVEL = 8, F_PUB = 9, F_STAT = 10, F_SCALE = 11, F_LW = 12 };

__device__ __forceinline__ float dot2(unsigned k, unsigned q, float acc) {
  return __builtin_amdgcn_fdot2_f32_bf16(__builtin_bit_cast(bf16x2, k), __builtin_bit_cast(bf16x2, q), acc, false);
}
template <int CTRL> __device__ __forceinline__ float dpp(float x) {
  return __builtin_bit_cast(float, __builtin_amdgcn_mov_dpp(__builtin_bit_cast(int, x), CTRL, 0xf, 0xf, true));
}
template <int CTRL> __device__ __forceinline__ int dppi(int x) { return __builtin_amdgcn_mov_dpp(x, CTRL, 0xf, 0xf, true); }
constexpr int XOR1 = 0xB1, XOR2 = 0x4E, XOR7 = 0x141, ROR4 = 0x124, ROR8 = 0x128, ROR12 = 0x12C, IDENT = 0xE4;
__device__ __forceinline__ unsigned cvtpk(float lo, float hi) {
  bf16x2 v = {(__bf16)lo, (__bf16)hi};
  return __builtin_bit_cast(unsigned, v);
}
__device__ __forceinline__ float bf16r(float x) { return __uint_as_float(cvtpk(x, x) << 16); }
__device__ __forceinline__ float xrow_max(float x) {
  auto s = __builtin_amdgcn_permlane16_swap(__float_as_uint(x), __float_as_uint(x), false, false);
  x = fmaxf(__uint_as_float(s[0]), __uint_as_float(s[1]));
  auto t = __builtin_amdgcn_permlane32_swap(__float_as_uint(x), __float_as_uint(x), false, false);
  return fmaxf(__uint_as_float(t[0]), __uint_as_float(t[1]));
}
__device__ __forceinline__ float xrow_sum(float x) {
  auto s = __builtin_amdgcn_permlane16_swap(__float_as_uint(x), __float_as_uint(x), false, false);
  x = __uint_as_float(s[0]) + __uint_as_float(s[1]);
  auto t = __builtin_amdgcn_permlane32_swap(__float_as_uint(x), __float_as_uint(x), false, false);
  return __uint_as_float(t[0]) + __uint_as_float(t[1]);
}
__device__ __forceinline__ f32x4 mfma(u32x4 a, u32x4 b, f32x4 c) {
  return __builtin_amdgcn_mfma_f32_16x16x32_bf16(__builtin_bit_cast(bf16x8, a), __builtin_bit_cast(bf16x8, b), c, 0, 0, 0);
}
__device__ __forceinline__ void put_granule(unsigned char* ws, unsigned off, unsigned tag, unsigned v) {
  __hip_atomic_store((GAS u64*)(ws + off), ((u64)tag << 32) | (u64)v, RLX_AGENT);
}
__device__ __forceinline__ unsigned duplo(unsigned x) { return __builtin_amdgcn_perm(x, x, 0x01000100u); }
__device__ __forceinline__ unsigned duphi(unsigned x) { return __builtin_amdgcn_perm(x, x, 0x03020302u); }
__device__ __forceinline__ unsigned lds_arrive(LAS unsigned* c, int lane) {
  asm volatile("s_waitcnt lgkmcnt(0)" ::: "memory");
  unsigned r = 0;
  if (lane == 0) r = __hip_atomic_fetch_add(c, 1u, RLX_WG);
  r = __builtin_amdgcn_readfirstlane(r);
  asm volatile("" ::: "memory");
  return r;
}
__device__ __forceinline__ bool lds_reduce4(float* buf, int slot, LAS unsigned* c, int lane, f32x4 v, f32x4& sum) {
  *(f32x4*)(buf + slot * 256 + lane * 4) = v;
  asm volatile("" ::: "memory");
  unsigned z = 0, r = 0;
  asm volatile("" : "+v"(z));
  if (lane == 0) r = __hip_atomic_fetch_add(c + z, 1u, RLX_WG);
  asm volatile("" ::: "memory");
  const f32x4 a = *(const f32x4*)(buf + lane * 4), b = *(const f32x4*)(buf + 256 + lane * 4);
  const f32x4 d = *(const f32x4*)(buf + 512 + lane * 4), e = *(const f32x4*)(buf + 768 + lane * 4);
  sum = (a + b) + (d + e);
  asm volatile("" : "+v"(sum) :: "memory");
  return __builtin_amdgcn_readfirstlane(r) == 3;
}
__device__ __forceinline__ void lds_set(LAS unsigned* c, unsigned v, int lane) {
  asm volatile("s_waitcnt lgkmcnt(0)" ::: "memory");
  if (lane == 0) __hip_atomic_store(c, v, RLX_WG);
}
template <int SLEEP> __device__ __forceinline__ void lds_wait(LAS unsigned* c, unsigned want) {
#pragma nounroll
  for (unsigned spins = 0; spins < (1u << 25) / (SLEEP + 2); ++spins) {
    if (__builtin_amdgcn_readfirstlane(__hip_atomic_load(c, RLX_WG)) >= want) break;
    __builtin_amdgcn_s_sleep(SLEEP);
  }
  asm volatile("" ::: "memory");
}
#define RSRC(p, n) __builtin_amdgcn_make_buffer_rsrc((void*)(p), 0, (n), 0x00020000)
#define LD128(r, off, aux) __builtin_amdgcn_raw_buffer_load_b128((r), (off), 0, (aux))
#define LD64(r, off, aux) __builtin_amdgcn_raw_buffer_load_b64((r), (off), 0, (aux))
#define LD32(r, off, aux) __builtin_amdgcn_raw_buffer_load_b32((r), (off), 0, (aux))
#define SCHED() __builtin_amdgcn_sched_barrier(0)
#define ST2(off, va, vb) __builtin_amdgcn_raw_buffer_store_b128(u32x4{__float_as_uint(va), tag, __float_as_uint(vb), tag}, wsr, (off), 0, 16)
constexpr int NT_ = 2, SC1 = 16;

__global__ void __launch_bounds__(512, 2)
fused_decode(const unsigned short* __restrict__ wq, const unsigned short* __restrict__ x, unsigned char* ws,
             const unsigned short* __restrict__ kc, const unsigned short* __restrict__ vc,
             const unsigned short* __restrict__ wg, const unsigned short* __restrict__ wu, const unsigned short* __restrict__ gw,
             const unsigned short* __restrict__ wo, const unsigned short* __restrict__ wd, unsigned short* __restrict__ out) {
  __shared__ __attribute__((aligned(16))) float lds[3 * 1024 + 128 + 32 + 16 + 256 + 4 * 4096];
  float* red_f = lds;
  float* mrg = lds + 1024;
  float* red_o = lds + 2048;
  unsigned* qbuf = (unsigned*)(lds + 3072);
  float* ml = lds + 3072 + 128;
  LAS unsigned* flg = (LAS unsigned*)(lds + 3072 + 128 + 32);
  LAS unsigned* prog = (LAS unsigned*)(lds + 3072 + 128 + 32 + 16);
  float* sbuf = lds + 3072 + 128 + 32 + 16 + 256;

  const int bid = blockIdx.x, tid = threadIdx.x, lane = tid & 63;
  const int wave = __builtin_amdgcn_readfirstlane(tid >> 6);
  const int g = lane >> 4, i = lane & 15;
  const int sel = (i >> 1) & 1;
  const int jj = 2 * (i >> 2) + (i & 1);
  const int grp = bid >> 3, mem = bid & 7;
  asm volatile("" :: "s"(wq), "s"(x), "s"(ws), "s"(kc), "s"(vc), "s"(wg), "s"(wu), "s"(gw), "s"(wo), "s"(wd), "s"(out));

  const __amdgpu_buffer_rsrc_t wsr = RSRC(ws, WS_END);
  const unsigned lo = lane * 16;
  const unsigned hmask = (i & 1) ? 0xffff0000u : 0x0000ffffu;

  if (__builtin_expect(wave < 4, 1)) {
    const __amdgpu_buffer_rsrc_t kr = RSRC((const char*)kc + ((size_t)bid << 20), 1u << 20);
    const __amdgpu_buffer_rsrc_t vr = RSRC((const char*)vc + ((size_t)bid << 20), 1u << 20);
    u32x4 r[32];
    const unsigned off = lo + (unsigned)wave * 4096u;
#define LDR(j, rs, o) (r[j] = LD128(rs, (o) + ((j) >> 2) * 16384u + ((j) & 3) * 1024u, NT_))
    u32x4 q0, q1;
    unsigned tag;
#pragma unroll
    for (int j = 0; j < 32; ++j) {
      if (j == PWIN) { __builtin_amdgcn_s_barrier(); }
      if (j == QAT) {
        lds_wait<1>(flg + F_Q, 1u);
        q0 = *(const u32x4*)(qbuf + 4 * i);
        q1 = *(const u32x4*)(qbuf + 64 + 4 * i);
        tag = flg[F_TAG];
      }
      if (j >= PWIN) { asm volatile("s_waitcnt vmcnt(%0)" :: "n"(PWIN - 1) : "memory"); }
      SCHED();
      LDR(j, kr, off);
      SCHED();
    }

    constexpr float SCALE = 0.08838834764831845f, LOG2E = 1.4426950408889634f;
    float* sp = sbuf + wave * 4096 + lane;
    float m = -INFINITY, l = 0.f;
    float xx[8];
#define KSTEP(t_, kreg) do { float d0 = 0.f, d1 = 0.f; \
      _Pragma("unroll") for (int k_ = 0; k_ < 4; ++k_) { d0 = dot2((kreg)[k_], q0[k_], d0); d1 = dot2((kreg)[k_], q1[k_], d1); } \
      xx[(t_) * 2] = d0; xx[(t_) * 2 + 1] = d1; } while (0)
#define SCORE(dst) do { \
      _Pragma("unroll") for (int h_ = 0; h_ < 4; ++h_) { const float lo_ = xx[h_], hi_ = xx[h_ + 4]; const float t1 = lo_ + dpp<ROR8>(lo_), t2 = hi_ + dpp<ROR8>(hi_); xx[h_] = (i & 8) ? t2 : t1; } \
      _Pragma("unroll") for (int h_ = 0; h_ < 2; ++h_) { const float lo_ = xx[h_], hi_ = xx[h_ + 2]; const float t1 = lo_ + dpp<XOR7>(lo_), t2 = hi_ + dpp<XOR7>(hi_); xx[h_] = (i & 4) ? t2 : t1; } \
      { const float lo_ = xx[0], hi_ = xx[1]; const float t1 = lo_ + dpp<XOR2>(lo_), t2 = hi_ + dpp<XOR2>(hi_); xx[0] = (i & 2) ? t2 : t1; } \
      const float sc = bf16r(bf16r(xx[0] + dpp<XOR1>(xx[0])) * SCALE) * LOG2E; \
      const float mn_ = fmaxf(m, sc); l = fmaf(l, __builtin_amdgcn_exp2f(m - mn_), __builtin_amdgcn_exp2f(sc - mn_)); m = mn_; (dst) = sc; } while (0)
    unsigned dnext = 2;
    int dl = 2;
#define PACE(step) do { dl = __builtin_amdgcn_readfirstlane(dnext); dnext = __hip_atomic_load(flg + F_LW + wave, RLX_WG); \
      __hip_atomic_store(prog + wave * 64 + lane, (unsigned)(step), RLX_WG); SCHED(); } while (0)
#define GW(n, c) "s_cmp_lt_i32 %0, " #c "\n\ts_cbranch_scc1 1f\n\ts_waitcnt vmcnt(%" #n ")\n\t"
#define GATE() asm volatile("s_waitcnt vmcnt(%1)\n\t" GW(2, 1) GW(3, 2) GW(4, 3) GW(5, 4) "1:" \
      :: "s"(dl), "n"(NWIN + 1), "n"(NWIN), "n"(NWIN - 1), "n"(NWIN - 2), "n"(NWIN - 3) : "scc")
#define KTURN(j) KSTEP((j) & 3, r[j]); asm volatile("" : "+v"(xx[2 * ((j) & 3)]), "+v"(xx[2 * ((j) & 3) + 1])); SCHED(); GATE(); SCHED(); LDR(j, nr, no); SCHED();
#define KTILE(u) KTURN(4 * (u)) KTURN(4 * (u) + 1) KTURN(4 * (u) + 2) KTURN(4 * (u) + 3) SCORE(sp[(t + (u)) * 64]); SCHED();
#pragma nounroll
    for (int t = 0; t < NTILE; t += 8) {
      const bool last = t + 8 >= NTILE;
      const __amdgpu_buffer_rsrc_t nr = last ? vr : kr;
      const unsigned no = last ? off : off + (unsigned)(t + 8) * 16384u;
      PACE(t); KTILE(0) PACE(t + 1); KTILE(1) PACE(t + 2); KTILE(2) PACE(t + 3); KTILE(3) PACE(t + 4); KTILE(4) PACE(t + 5); KTILE(5) PACE(t + 6); KTILE(6) PACE(t + 7); KTILE(7)
    }
    {
      float mw = fmaxf(m, dpp<ROR4>(m)); mw = fmaxf(mw, dpp<ROR8>(mw)); mw = xrow_max(mw);
      l *= __builtin_amdgcn_exp2f(m - mw); m = mw;
      l += dpp<ROR4>(l); l += dpp<ROR8>(l); l = xrow_sum(l);
      if (lane == 2 * sel) *(float2*)(ml + (wave * 2 + sel) * 2) = make_float2(m, l);
      asm volatile("s_waitcnt lgkmcnt(0)" ::: "memory");
      if (lane == 0) __hip_atomic_fetch_add(flg + F_STAT, 1u, RLX_WG);
    }
    f32x4 D = {0.f, 0.f, 0.f, 0.f};
    u32x4 pf = {0u, 0u, 0u, 0u};
    unsigned pq;
    float cw = 0.f;
    float s0 = sp[0];
#define PROB(sv) (cvtpk(__builtin_amdgcn_exp2f((sv) - m), __builtin_amdgcn_exp2f((sv) - m)) & hmask)
#define VSTEP(s_, vreg, pf, pq, D) do { \
      pf[(s_) & 3] = __builtin_amdgcn_update_dpp(pf[(s_) & 3], pq, IDENT, 0xf, 1 << ((s_) & 3), false); \
      pf[((s_) + 1) & 3] = __builtin_amdgcn_update_dpp(pf[((s_) + 1) & 3], pq, ROR4, 0xf, 1 << (((s_) + 1) & 3), false); \
      pf[((s_) + 2) & 3] = __builtin_amdgcn_update_dpp(pf[((s_) + 2) & 3], pq, ROR8, 0xf, 1 << (((s_) + 2) & 3), false); \
      pf[((s_) + 3) & 3] = __builtin_amdgcn_update_dpp(pf[((s_) + 3) & 3], pq, ROR12, 0xf, 1 << (((s_) + 3) & 3), false); \
      D = mfma(vreg, pf, D); } while (0)
#define VTURN(j) VSTEP((j) & 3, r[j], pf, pq, D); asm volatile("" : "+v"(D)); SCHED(); GATE(); SCHED(); LDR(j, vr, no); SCHED();
#define VTILE(u) pq = PROB(s0); s0 = sp[(t + (u) + 1) * 64]; SCHED(); VTURN(4 * (u)) VTURN(4 * (u) + 1) VTURN(4 * (u) + 2) VTURN(4 * (u) + 3)
    unsigned pl[8];
    unsigned apo = WS_AP + (unsigned)((2 * (grp & 15) + sel) * 4 + g) * 2048u + (unsigned)((mem & 1) | ((grp >> 4) << 1) | ((mem >> 1) << 2)) * 128u + jj * 16u;
    asm volatile("" : "+v"(apo));
#pragma nounroll
    for (int t = 0; t < NTILE - 8; t += 8) {
      const unsigned no = off + (unsigned)(t + 8) * 16384u;
      if (t == NTILE - 16) {
        lds_wait<1>(flg + F_SCALE, 1u);
        cw = ml[16 + wave * 2 + sel];
      }
      PACE(NTILE + t); VTILE(0) PACE(NTILE + t + 1); VTILE(1) PACE(NTILE + t + 2); VTILE(2) PACE(NTILE + t + 3); VTILE(3)
      PACE(NTILE + t + 4); VTILE(4) PACE(NTILE + t + 5); VTILE(5) PACE(NTILE + t + 6); VTILE(6) PACE(NTILE + t + 7); VTILE(7)
    }
    {
      PACE(2 * NTILE - 8);
#pragma unroll
      for (int u = 0; u < 8; ++u) pl[u] = PROB(sp[(NTILE - 8 + u) * 64]);
      f32x4 E = {0.f, 0.f, 0.f, 0.f};
      u32x4 pg = {0u, 0u, 0u, 0u};
#define VLAST(u) VSTEP(0, r[4 * (u)], pf, pl[u], D); VSTEP(1, r[4 * (u) + 1], pg, pl[u], E); VSTEP(2, r[4 * (u) + 2], pf, pl[u], D); VSTEP(3, r[4 * (u) + 3], pg, pl[u], E);
      VLAST(0) VLAST(1) VLAST(2) VLAST(3) VLAST(4) VLAST(5) VLAST(6) VLAST(7)
      D += E;
    }
    f32x4 o;
    if (lds_reduce4(mrg, wave, flg + F_MERGE, lane, D * cw, o)) {
      ST2(apo, __uint_as_float(cvtpk(o[0], o[1])), __uint_as_float(cvtpk(o[2], o[3])));
      lds_set(flg + F_PUB, 1u, lane);
    }
    if (wave == 0 && bid < 72) {
      lds_wait<1>(flg + F_OUT, 1u);
      __builtin_amdgcn_s_sleep(24);
      const unsigned ro = WS_OP + (unsigned)bid * 4096u + lo;
      u32x4 rv[4];
#pragma unroll
      for (int k = 0; k < 4; ++k) rv[k] = LD128(wsr, ro + k * 1024u, SC1);
#define R_OK(k) (rv[k][1] == tag && rv[k][3] == tag)
      for (unsigned spins = 0; spins < SPIN_CAP; ++spins) {
        if (__all(R_OK(0) && R_OK(1) && R_OK(2) && R_OK(3))) break;
        asm volatile("" ::: "memory");
#pragma unroll
        for (int k = 0; k < 4; ++k) if (!R_OK(k)) rv[k] = LD128(wsr, ro + k * 1024u, SC1);
      }
      float va = (__uint_as_float(rv[0][0]) + __uint_as_float(rv[1][0])) + (__uint_as_float(rv[2][0]) + __uint_as_float(rv[3][0]));
      float vb = (__uint_as_float(rv[0][2]) + __uint_as_float(rv[1][2])) + (__uint_as_float(rv[2][2]) + __uint_as_float(rv[3][2]));
      va += dpp<ROR8>(va); vb += dpp<ROR8>(vb);
      va = xrow_sum(va); vb = xrow_sum(vb);
      if (lane < 16) __hip_atomic_store((GAS unsigned short*)out + 16 * bid + lane, (unsigned short)cvtpk(lane < 8 ? va : vb, 0.f), RLX_AGENT);
      if (bid == 0 && lane < 2) __hip_atomic_store((GAS unsigned*)(ws + (lane ? WS_EPOCH2 : WS_EPOCH)), tag, RLX_AGENT);
    }
    return;
  }

  const int sw = wave - 4;
  const __amdgpu_buffer_rsrc_t xr = RSRC(x, 2 * DM * 2);
  u32x4 msk;
#pragma unroll
  for (int k = 0; k < 4; ++k) msk[k] = ((i >> 2) == k) ? hmask : 0u;
#define BFRAG(dup) (u32x4{(dup) & msk[0], (dup) & msk[1], (dup) & msk[2], (dup) & msk[3]})
  unsigned tag;

  if (sw == 0) {
    __builtin_amdgcn_s_setprio(3);
    const __amdgpu_buffer_rsrc_t wqr = RSRC(wq, DM * 4096 * 2);
    u32x4 aq[18];
    const unsigned r0 = 72 * mem + 18 * g;
#pragma unroll
    for (int st = 0; st < 18; ++st) aq[st] = LD128(wqr, (r0 + st) * 8192u + (unsigned)grp * 256u + 16u * i, NT_);
    const unsigned xo = (sel * DM + r0) * 2u;
    const u32x4 x0 = LD128(xr, xo, 0), x1 = LD128(xr, xo + 16, 0);
    const unsigned x2 = LD32(xr, xo + 32, 0);
    const unsigned ep1 = LD32(wsr, WS_EPOCH, SC1), ep2 = LD32(wsr, WS_EPOCH2, SC1);
    if (lane < 16) flg[lane] = (lane == F_LEVEL || lane >= F_LW) ? 2u : 0u;
    prog[lane] = 0u; prog[64 + lane] = 0u; prog[128 + lane] = 0u; prog[192 + lane] = 0u;
    SCHED();
    asm volatile("s_waitcnt lgkmcnt(0)" ::: "memory");
    __builtin_amdgcn_s_barrier();
    SCHED();
    tag = (ep1 + 2u * ep2 + 0x9E3779B9u) | 1u;
    if (tag == ep1 || tag == ep2) tag += 2u;
    if (tag == ep1 || tag == ep2) tag += 2u;
    if (lane == 0) flg[F_TAG] = tag;
    lds_set(flg + F_TAGOK, 1u, lane);
    const unsigned xs[9] = {x0[0], x0[1], x0[2], x0[3], x1[0], x1[1], x1[2], x1[3], x2};
    f32x4 d0 = {0.f, 0.f, 0.f, 0.f}, d1 = {0.f, 0.f, 0.f, 0.f};
#pragma unroll
    for (int k = 0; k < 9; ++k) { d0 = mfma(aq[2 * k], BFRAG(duplo(xs[k])), d0); d1 = mfma(aq[2 * k + 1], BFRAG(duphi(xs[k])), d1); }
    d0 += d1;
    {
      const unsigned qb = WS_QP + (unsigned)(((sel * 16 + (grp >> 1)) * 2 + (grp & 1)) * 8 + mem) * 1024u + (unsigned)(8 * g + jj) * 16u;
      ST2(qb, d0[0], d0[1]);
      ST2(qb + 512u, d0[2], d0[3]);
    }
    __builtin_amdgcn_s_sleep(QDELAY);
    u32x4 qa[16];
    const unsigned qoff = WS_QP + (unsigned)grp * 16384u + lo;
    asm volatile("" ::: "memory");
#pragma unroll
    for (int k = 0; k < 16; ++k) qa[k] = LD128(wsr, qoff + k * 1024u, SC1);
    for (unsigned spins = 0; spins < SPIN_CAP; ++spins) {
      unsigned bad = 0;
#pragma unroll
      for (int k = 0; k < 16; ++k) bad |= (qa[k][1] != tag || qa[k][3] != tag) ? (1u << k) : 0u;
      if (__all(bad == 0)) break;
      asm volatile("" ::: "memory");
#pragma unroll
      for (int k = 0; k < 16; ++k) if ((bad >> k) & 1) qa[k] = LD128(wsr, qoff + k * 1024u, SC1);
    }
    float v[4] = {0.f, 0.f, 0.f, 0.f};
#pragma unroll
    for (int s = 0; s < 8; ++s) {
      v[0] += __uint_as_float(qa[s][0]); v[1] += __uint_as_float(qa[s][2]);
      v[2] += __uint_as_float(qa[8 + s][0]); v[3] += __uint_as_float(qa[8 + s][2]);
    }
    {
      unsigned short* qs = (unsigned short*)qbuf + 32 * ((lane >> 3) & 3) + 16 * (lane >> 5) + (lane & 7);
      const unsigned p01 = cvtpk(v[0], v[1]), p23 = cvtpk(v[2], v[3]);
      qs[0] = (unsigned short)p01; qs[8] = (unsigned short)(p01 >> 16);
      qs[128] = (unsigned short)p23; qs[136] = (unsigned short)(p23 >> 16);
    }
    lds_set(flg + F_Q, 1u, lane);
    __builtin_amdgcn_s_setprio(0);
  } else {
    const bool has_f = mem < 6 || (mem == 6 && (grp & 3) != 3);
    const int u = mem < 6 ? grp * 6 + mem : 192 + grp - (grp >> 2);
    const int fmat = u >= 108 ? 1 : 0, fcb = (u - 108 * fmat) / 3, fkt = (u - 108 * fmat) % 3;
    if (has_f) {
      const __amdgpu_buffer_rsrc_t wfr = RSRC(fmat ? wu : wg, DM * FF * 2);
      const unsigned r0 = 192 * fkt + 64 * (sw - 1) + 16 * g;
      const u32x4 xa = LD128(xr, (sel * DM + r0) * 2u, 0), xb = LD128(xr, (sel * DM + r0) * 2u + 16, 0);
      const unsigned xs[8] = {xa[0], xa[1], xa[2], xa[3], xb[0], xb[1], xb[2], xb[3]};
      const unsigned fo = r0 * (FF * 2u) + (unsigned)fcb * 256u + 16u * i;
      u32x4 af[16];
#pragma unroll
      for (int st = 0; st < FWIN; ++st) af[st] = LD128(wfr, fo + st * (FF * 2u), NT_);
      SCHED();
      __builtin_amdgcn_s_barrier();
      SCHED();
      f32x4 d0 = {0.f, 0.f, 0.f, 0.f}, d1 = {0.f, 0.f, 0.f, 0.f};
#pragma unroll
      for (int st = 0; st < 16; ++st) {
        asm volatile("" : "+v"(af[st]));
        SCHED();
        if (st + FWIN < 16) af[st + FWIN] = LD128(wfr, fo + (st + FWIN) * (FF * 2u), NT_);
        SCHED();
        if (st & 1) d1 = mfma(af[st], BFRAG(duphi(xs[st >> 1])), d1); else d0 = mfma(af[st], BFRAG(duplo(xs[st >> 1])), d0);
      }
      *(f32x4*)(red_f + (sw - 1) * 256 + lane * 4) = d0 + d1;
      lds_wait<1>(flg + F_TAGOK, 1u);
      tag = flg[F_TAG];
      if (lds_arrive(flg + F_FFN, lane) == 2) {
        const f32x4 s = *(const f32x4*)(red_f + lane * 4) + *(const f32x4*)(red_f + 256 + lane * 4) + *(const f32x4*)(red_f + 512 + lane * 4);
        const unsigned fb = WS_FP + (unsigned)(((fcb * 2 + fmat) * 3 + fkt) * 2 + sel) * 1024u + (unsigned)(8 * g + jj) * 16u;
        ST2(fb, s[0], s[1]);
        ST2(fb + 512u, s[2], s[3]);
      }
    } else {
      __builtin_amdgcn_s_barrier();
      asm volatile("" ::: "memory");
      lds_wait<1>(flg + F_TAGOK, 1u);
      tag = flg[F_TAG];
    }
  }

  const int hidx = bid & 31, jn = bid >> 5;
  const bool wide = jn == 7;
  u32x4 ao[8], ad[9];
  float gate;
  const __amdgpu_buffer_rsrc_t wor = RSRC(wo, 4096 * DM * 2);
  const __amdgpu_buffer_rsrc_t wdr = RSRC(wd, FF * DM * 2);
  const unsigned co = (i < 8 || wide) ? 128u * jn + 16u * i : OOB;
  const unsigned dof = co + (unsigned)(144 * hidx + 36 * sw + g) * (DM * 2u), oof = co + (unsigned)(128 * hidx + 32 * sw + g) * (DM * 2u);
  {
    const __amdgpu_buffer_rsrc_t gwr = RSRC(gw, DM * 2);
    u32x4 gx[3], gg[3];
#pragma unroll
    for (int k = 0; k < 3; ++k) {
      const unsigned ch = (unsigned)(lane & 31) + 32u * k;
      gx[k] = LD128(xr, ch < 72 ? (unsigned)(lane >> 5) * (DM * 2u) + ch * 16u : OOB, 0);
      gg[k] = LD128(gwr, ch * 16u, 0);
    }
    float s = 0.f;
#pragma unroll
    for (int k = 0; k < 3; ++k)
#pragma unroll
      for (int e = 0; e < 4; ++e)
        s += bf16r(__uint_as_float(gx[k][e] << 16) * __uint_as_float(gg[k][e] << 16)) +
             bf16r(__uint_as_float(gx[k][e] & 0xffff0000u) * __uint_as_float(gg[k][e] & 0xffff0000u));
    s += dpp<XOR1>(s); s += dpp<XOR2>(s); s += dpp<ROR4>(s); s += dpp<ROR8>(s);
    auto t = __builtin_amdgcn_permlane16_swap(__float_as_uint(s), __float_as_uint(s), false, false);
    s = __uint_as_float(t[0]) + __uint_as_float(t[1]);
    const float s0 = __builtin_bit_cast(float, __builtin_amdgcn_readlane(__builtin_bit_cast(int, s), 0));
    const float s1 = __builtin_bit_cast(float, __builtin_amdgcn_readlane(__builtin_bit_cast(int, s), 32));
    gate = bf16r(1.f / (1.f + __expf(-bf16r(sel ? s1 : s0))));
    asm volatile("" : "+v"(gate));
#pragma unroll
    for (int st = 0; st < 9; ++st) ad[st] = LD128(wdr, wide ? dof + st * (4 * DM * 2u) : OOB, NT_);
    const unsigned t0 = LD32(wdr, lane == 0 ? dof : OOB, NT_);
    const unsigned t1 = LD32(wor, lane == 0 ? oof : OOB, NT_);
    asm volatile("" :: "v"(t0), "v"(t1));
  }
  if (sw == 1) {
    u32x2 fpv[5];
    unsigned fpo[5];
#pragma unroll
    for (int st = 0; st < 5; ++st) {
      const int rl = 4 * st + g;
      const unsigned f = 18 * bid + rl, n = f & 127;
      fpo[st] = (rl < 18 && (i >> 2) < 3) ? WS_FP + (unsigned)((((f >> 7) * 2 + (i & 1)) * 3 + (i >> 2)) * 2 + sel) * 1024u +
                                            ((n >> 4) & 1) * 512u + (8 * (n >> 5) + (n & 7)) * 16u + ((n >> 3) & 1) * 8u : OOB;
    }
#define FP_OK() ((fpo[0] == OOB || fpv[0][1] == tag) && (fpo[1] == OOB || fpv[1][1] == tag) && (fpo[2] == OOB || fpv[2][1] == tag) && \
                 (fpo[3] == OOB || fpv[3][1] == tag) && (fpo[4] == OOB || fpv[4][1] == tag))
    lds_wait<32>(prog, 16u);
    unsigned spins = 0;
    do {
      if (spins) __builtin_amdgcn_s_sleep(64);
      asm volatile("" ::: "memory");
#pragma unroll
      for (int st = 0; st < 5; ++st) fpv[st] = LD64(wsr, fpo[st], SC1);
    } while (!__all(FP_OK()) && ++spins < SPIN_CAP);
#pragma unroll
    for (int st = 0; st < 5; ++st) {
      float s = __uint_as_float(fpv[st][0]);
      s += dpp<ROR4>(s); s += dpp<ROR8>(s);
      const float o = dpp<XOR1>(s);
      const float gv = bf16r((i & 1) ? o : s), uv = bf16r((i & 1) ? s : o);
      const float z2 = 1.5957691216057308f * (gv + 0.044715f * gv * gv * gv);
      const float hv = bf16r(gv / (1.f + __expf(-z2))) * uv;
      const float ho = dpp<XOR2>(hv);
      if (i == 0 && 4 * st + g < 18) put_granule(ws, WS_HP + (unsigned)(18 * bid + 4 * st + g) * 8u, tag, cvtpk(hv, ho));
    }
  }
  unsigned hval = 0;
  bool hgot = false;
#define HFETCH() do { \
    const unsigned hpo = lane < 36 ? WS_HP + (unsigned)(144 * hidx + 36 * sw + lane) * 8u : OOB; \
    u32x2 hpv; unsigned sp_ = 0; \
    do { if (sp_) __builtin_amdgcn_s_sleep(64); asm volatile("" ::: "memory"); hpv = LD64(wsr, hpo, SC1); \
    } while (!__all(lane >= 36 || hpv[1] == tag) && ++sp_ < SPIN_CAP); \
    hval = hpv[0]; hgot = true; } while (0)
  if (sw == 0) {
    const unsigned peer = (lane < 8 && lane != mem) ? WS_PG + (unsigned)((bid + 33 * (lane - mem)) & 255) * 128u : OOB;
#pragma nounroll
    for (unsigned spins = 0; spins < SPIN_CAP; ++spins) {
      if (__builtin_amdgcn_readfirstlane(__hip_atomic_load(prog, RLX_WG)) >= HT3) break;
      if (!hgot && __builtin_amdgcn_readfirstlane(__hip_atomic_load(prog, RLX_WG)) >= 40) HFETCH();
      int p = (int)__hip_atomic_load(prog + (lane & 3) * 64, RLX_WG);
      p += dppi<XOR1>(p); p += dppi<XOR2>(p);
      const unsigned e = (unsigned)p * PTAU - (unsigned)__builtin_amdgcn_s_memrealtime();
      if (lane == 0) put_granule(ws, WS_PG + (unsigned)bid * 128u, tag, e);
      asm volatile("" ::: "memory");
      const u32x2 t = LD64(wsr, peer, SC1);
      int df = t[1] == tag ? (int)(e - t[0]) : 0;
      df += dppi<XOR1>(df); df += dppi<XOR2>(df); df += dppi<XOR7>(df);
      const int lead = __builtin_amdgcn_readfirstlane(df) >> 3;
      const int psl = __builtin_amdgcn_readfirstlane(p) < 4 * PSWITCH ? PSL0 : PSL;
      const int ns = min((abs(lead) * PGAIN) >> 8, psl);
      if (lane == 0) __hip_atomic_store(flg + F_LEVEL, ns == 0 ? 2u : lead < -BIGL ? 0u : lead < 0 ? 1u : lead > BIGL ? 4u : 3u, RLX_WG);
#pragma nounroll
      for (int k = 0; k < psl; ++k) {
        if (k == ns && lane == 0) __hip_atomic_store(flg + F_LEVEL, 2u, RLX_WG);
        __builtin_amdgcn_s_sleep(PSLICE);
        if (__builtin_amdgcn_readfirstlane(__hip_atomic_load(prog, RLX_WG)) >= HT4) break;
      }
    }
    if (lane == 0) __hip_atomic_store(flg + F_LEVEL, 2u, RLX_WG);
  }
  if (sw == 3) {
#pragma nounroll
    for (unsigned spins = 0; spins < (1u << 20); ++spins) {
      const int p = (int)__hip_atomic_load(prog + (lane & 3) * 64, RLX_WG);
      const int base = (int)__hip_atomic_load(flg + F_LEVEL, RLX_WG);
      int sum = p + dppi<XOR1>(p); sum += dppi<XOR2>(sum);
      const int d4 = 4 * p - sum;
      const int lv = min(max(base + (d4 >= WBAND ? 1 : d4 <= -WBAND ? -1 : 0), 0), 4);
      if (lane < 4) __hip_atomic_store(flg + F_LW + lane, (unsigned)lv, RLX_WG);
      if (__builtin_amdgcn_readfirstlane(sum) >= 4 * HT4) break;
      if (!hgot && __builtin_amdgcn_readfirstlane(sum) >= 4 * 40) HFETCH();
      __builtin_amdgcn_s_sleep(WSLEEP);
    }
  }
  if (!hgot) { lds_wait<64>(prog, 36u); HFETCH(); }
  if (sw == 2) {
    lds_wait<16>(flg + F_STAT, 4u);
    const int h = (lane >> 3) & 1;
    const float2 e = *(const float2*)(ml + ((lane & 3) * 2 + h) * 2);
    {
      float M = fmaxf(e.x, dpp<XOR1>(e.x)); M = fmaxf(M, dpp<XOR2>(M));
      float L = e.y * __builtin_amdgcn_exp2f(e.x - M); L += dpp<XOR1>(L); L += dpp<XOR2>(L);
      ST2(lane == 8 * h ? WS_ML + (unsigned)bid * 128u + h * 16u : OOB, M, L);
    }
    lds_wait<32>(prog, NTILE + 16);
    const unsigned mo = lane < 16 ? WS_ML + (unsigned)(grp * 8 + (lane & 7)) * 128u + h * 16u : OOB;
    u32x4 t;
    unsigned spins = 0;
    do {
      if (spins) __builtin_amdgcn_s_sleep(32);
      asm volatile("" ::: "memory");
      t = LD128(wsr, mo, SC1);
    } while (!__all(lane >= 16 || (t[1] == tag && t[3] == tag)) && ++spins < SPIN_CAP);
    const float ms = __uint_as_float(t[0]);
    float M = fmaxf(ms, dpp<XOR1>(ms)); M = fmaxf(M, dpp<XOR2>(M)); M = fmaxf(M, dpp<XOR7>(M));
    float L = __uint_as_float(t[2]) * __builtin_amdgcn_exp2f(ms - M); L += dpp<XOR1>(L); L += dpp<XOR2>(L); L += dpp<XOR7>(L);
    if (lane < 16 && (lane & 7) < 4) ml[16 + (lane & 3) * 2 + h] = __builtin_amdgcn_exp2f(e.x - M) / L;
    lds_set(flg + F_SCALE, 1u, lane);
  }
  unsigned hd[9];
#pragma unroll
  for (int st = 0; st < 9; ++st) { const unsigned hh = __builtin_amdgcn_ds_bpermute((4 * st + g) * 4, hval); hd[st] = sel ? duphi(hh) : duplo(hh); }
  f32x4 Df = {0.f, 0.f, 0.f, 0.f};
#pragma unroll
  for (int st = 0; st < 9; ++st) Df = mfma(ad[st], BFRAG(hd[st]), Df);
  asm volatile("" : "+v"(Df));
  const unsigned dof2 = wide ? OOB : dof;
  lds_wait<32>(prog, NTILE + 40);
  lds_wait<2>(prog, NTILE + 56);
#pragma unroll
  for (int st = 0; st < 9; ++st) ad[st] = LD128(wdr, dof2 + st * (4 * DM * 2u), 0);
#pragma unroll
  for (int st = 0; st < 8; ++st) ao[st] = LD128(wor, oof + st * (4 * DM * 2u), 0);
  SCHED();
  lds_wait<1>(flg + F_PUB, 1u);
  __builtin_amdgcn_s_sleep(10);
  const unsigned aoff = WS_AP + (unsigned)(hidx * 4 + sw) * 2048u + (unsigned)i * 128u + g * 16u;
  unsigned obo = (g < 2 || wide) ? WS_OP + (unsigned)(sel * 36 + 4 * jn + 2 * g) * 4096u + (unsigned)hidx * 128u + jj * 16u : OOB, obo2 = obo + 4096u;
  asm volatile("" : "+v"(obo), "+v"(obo2) :: "memory");
  u32x4 a0 = LD128(wsr, aoff, SC1), a1 = LD128(wsr, aoff + 64, SC1);
  SCHED();
#pragma unroll
  for (int st = 0; st < 9; ++st) Df = mfma(ad[st], BFRAG(hd[st]), Df);
  Df *= bf16r(1.f - gate);
  asm volatile("" : "+v"(Df));
  SCHED();
  f32x4 Da = {0.f, 0.f, 0.f, 0.f};
  {
#define A_OK() (a0[1] == tag && a0[3] == tag && a1[1] == tag && a1[3] == tag)
    bool ok = A_OK();
    if (!__all(ok)) {
      unsigned spins = 0;
      do {
        if (__popcll(__ballot(!ok)) > 8) __builtin_amdgcn_s_sleep(HSLEEP);
        asm volatile("" ::: "memory");
        if (!ok) { a0 = LD128(wsr, aoff, SC1); a1 = LD128(wsr, aoff + 64, SC1); }
        ok = A_OK();
      } while (!__all(ok) && ++spins < SPIN_CAP);
    }
    float o[8] = {__uint_as_float(a0[0] << 16), __uint_as_float(a1[0] << 16), __uint_as_float(a0[0] & 0xffff0000u), __uint_as_float(a1[0] & 0xffff0000u),
                  __uint_as_float(a0[2] << 16), __uint_as_float(a1[2] << 16), __uint_as_float(a0[2] & 0xffff0000u), __uint_as_float(a1[2] & 0xffff0000u)};
    f32x4 Db = {0.f, 0.f, 0.f, 0.f};
#pragma unroll
    for (int st = 0; st < 8; ++st) o[st] += dpp<XOR1>(o[st]);
    SCHED();
#pragma unroll
    for (int st = 0; st < 8; ++st) o[st] += dpp<ROR4>(o[st]);
    SCHED();
#pragma unroll
    for (int st = 0; st < 8; ++st) o[st] += dpp<ROR8>(o[st]);
    SCHED();
    u32x4 bf[8];
#pragma unroll
    for (int st = 0; st < 8; ++st) { const unsigned c_ = cvtpk(o[st], o[st]); bf[st] = BFRAG(c_); }
#pragma unroll
    for (int st = 0; st < 8; ++st) asm volatile("" : "+v"(bf[st]));
#pragma unroll
    for (int st = 0; st < 8; ++st) { if (st & 1) Db = mfma(ao[st], bf[st], Db); else Da = mfma(ao[st], bf[st], Da); }
    Da += Db;
  }
  f32x4 s;
  if (lds_reduce4(red_o, sw, flg + F_OUT, lane, Da * gate + Df, s)) {
    ST2(obo, s[0], s[1]);
    ST2(obo2, s[2], s[3]);
  }
}

extern "C" void kernel_launch(void* const* d_in, const int* in_sizes, int n_in,
                              void* d_out, int out_size, void* d_ws, size_t ws_size,
                              hipStream_t stream) {
  fused_decode<<<256, 512, 0, stream>>>((const unsigned short*)d_in[3], (const unsigned short*)d_in[0], (unsigned char*)d_ws,
                                        (const unsigned short*)d_in[1], (const unsigned short*)d_in[2],
                                        (const unsigned short*)d_in[6], (const unsigned short*)d_in[7], (const unsigned short*)d_in[9],
                                        (const unsigned short*)d_in[4], (const unsigned short*)d_in[8], (unsigned short*)d_out);
}
```
